```python
import torch

def _softmax(x, axis=-1):
    return torch.softmax(x, dim=axis)

def _gelu(x):
    return torch.nn.functional.gelu(x, approximate='tanh')

def _silu(x):
    return torch.nn.functional.silu(x)

def _sigmoid(x):
    return torch.sigmoid(x)

def _layernorm(x, axis=-1, eps=1e-05):
    m = x.mean(dim=axis, keepdim=True)
    v = x.var(dim=axis, keepdim=True, unbiased=False)
    return (x - m) / torch.sqrt(v + eps)

def _scatter_add(tgt, idx, src, axis=0):
    return tgt.clone().index_add_(axis, idx, src)

def _segment_sum(src, seg, n):
    out = torch.zeros((n,) + src.shape[1:], dtype=src.dtype)
    return out.index_add_(0, seg, src)

def _pad(x, pw):
    flat = tuple((v for pair in reversed(pw) for v in pair))
    return torch.nn.functional.pad(x, flat)
kernel_name = 'interleave_layers__linear_kernel_hedgehog__mixer_gmlp__prompt'

def setup_inputs():

    _w = lambda a: (lambda t: t.to(torch.bfloat16) if t.is_floating_point() else t)(torch.as_tensor(a))

    def _A_setup():
        B, S, D, H = (1, 2048, 640, 32)
        Dh = D // H
        return {'x': _w((lambda *s: torch.randn(*s, dtype=torch.bfloat16) * 0.05)(B, S, D)), 'wqkv': _w((lambda *s: torch.randn(*s, dtype=torch.bfloat16) * 0.4)(D, 3 * D)), 'w_phi': _w((lambda *s: torch.randn(*s, dtype=torch.bfloat16) * 0.4)(H, Dh, Dh) * Dh ** (-0.5)), 'wo': _w((lambda *s: torch.randn(*s, dtype=torch.bfloat16) * 0.05)(D, D))}
    _A_in = _A_setup()

    def _B_setup():
        B, S, D, F = (1, 2048, 640, 1536)
        Fh = F // 2
        return {'x': _w((lambda *s: torch.randn(*s, dtype=torch.bfloat16) * 0.05)(B, S, D)), 'w_in': _w((lambda *s: torch.randn(*s, dtype=torch.bfloat16) * 0.4)(D, 2 * Fh)), 'w_sp': _w(0.01 * (lambda *s: torch.randn(*s, dtype=torch.bfloat16) * 0.4)(S, S)), 'b_sp': _w(1.0 + 0.01 * (lambda *s: torch.randn(*s, dtype=torch.bfloat16) * 0.05)(S)), 'w_out': _w((lambda *s: torch.randn(*s, dtype=torch.bfloat16) * 0.05)(Fh, D))}
    _B_in = _B_setup()
    return {'A_x': _A_in['x'], 'A_wqkv': _A_in['wqkv'], 'A_w_phi': _A_in['w_phi'], 'A_wo': _A_in['wo'], 'B_x': _B_in['x'], 'B_w_in': _B_in['w_in'], 'B_w_sp': _B_in['w_sp'], 'B_b_sp': _B_in['b_sp'], 'B_w_out': _B_in['w_out']}

def reference(A_x, A_wqkv, A_w_phi, A_wo, B_x, B_w_in, B_w_sp, B_b_sp, B_w_out):

    def _A_ref(x, wqkv, w_phi, wo):
        B, S, D = x.shape
        H = 32
        Dh = D // H
        qkv = (lambda x, *d: x.permute(*d))((x @ wqkv).reshape(B, S, 3, H, Dh), 2, 0, 3, 1, 4)
        q, k, v = (qkv[0], qkv[1], qkv[2])
        pq = torch.einsum('bhsd,hdf->bhsf', q, w_phi)
        pk = torch.einsum('bhsd,hdf->bhsf', k, w_phi)
        phi_q = torch.concatenate([_softmax(pq, axis=-1), _softmax(-pq, axis=-1)], axis=-1)
        phi_k = torch.concatenate([_softmax(pk, axis=-1), _softmax(-pk, axis=-1)], axis=-1)
        kv = torch.einsum('bhsf,bhsd->bhfd', phi_k, v)
        z = torch.einsum('bhsf->bhf', phi_k)
        num = torch.einsum('bhqf,bhfd->bhqd', phi_q, kv)
        den = torch.einsum('bhqf,bhf->bhq', phi_q, z)[..., None] + 1e-06
        o = num / den
        return (lambda x, *d: x.permute(*d))(o, 0, 2, 1, 3).reshape(B, S, D) @ wo

    def _B_ref(x, w_in, w_sp, b_sp, w_out):
        B, S, D = x.shape
        F = w_in.shape[-1]
        Fh = F // 2
        z = _gelu(torch.einsum('bsd,df->bsf', x, w_in))
        z1 = z[..., :Fh].clone()
        z2 = _layernorm(z[..., Fh:], axis=-1)
        g = torch.einsum('ts,bsf->btf', w_sp, z2) + b_sp[None, :, None]
        return torch.einsum('bsf,fd->bsd', z1 * g, w_out)
    x = A_x
    x = x + _A_ref(_layernorm(x, axis=-1), A_wqkv, A_w_phi, A_wo)
    x = x + _B_ref(_layernorm(x, axis=-1), B_w_in, B_w_sp, B_b_sp, B_w_out)
    x = x + _A_ref(_layernorm(x, axis=-1), A_wqkv, A_w_phi, A_wo)
    x = x + _B_ref(_layernorm(x, axis=-1), B_w_in, B_w_sp, B_b_sp, B_w_out)
    return x
```

```cpp
#include <hip/hip_runtime.h>
#include <hip/hip_bf16.h>
#include <stdint.h>

typedef unsigned short u16;
typedef short bf16x8 __attribute__((ext_vector_type(8)));
typedef short s16x4 __attribute__((ext_vector_type(4)));
typedef float f32x4 __attribute__((ext_vector_type(4)));
typedef unsigned int u32x4 __attribute__((ext_vector_type(4)));
typedef unsigned int u32x2 __attribute__((ext_vector_type(2)));
#define LDS3(T, p) ((__attribute__((address_space(3))) T*)(p))

constexpr int S_ = 2048, D_ = 640, H_ = 32, DH_ = 20, FH_ = 768;

__device__ __forceinline__ float bf2f(u16 v) { return __uint_as_float(((unsigned)v) << 16); }
__device__ __forceinline__ u16 f2bf(float f) { return __builtin_bit_cast(u16, (__bf16)f); }
__device__ __forceinline__ float rbf(float f) { return bf2f(f2bf(f)); }
__device__ __forceinline__ f32x4 mfma16(bf16x8 a, bf16x8 b, f32x4 c) {
  return __builtin_amdgcn_mfma_f32_16x16x32_bf16(a, b, c, 0, 0, 0);
}
__device__ __forceinline__ s16x4 trread(const u16* lds_ptr) {
  return __builtin_amdgcn_ds_read_tr16_b64_v4i16(LDS3(s16x4, lds_ptr));
}
__device__ __forceinline__ bf16x8 cat4(s16x4 a, s16x4 b) {
  bf16x8 r; r[0]=a[0]; r[1]=a[1]; r[2]=a[2]; r[3]=a[3]; r[4]=b[0]; r[5]=b[1]; r[6]=b[2]; r[7]=b[3]; return r;
}

__device__ __forceinline__ float ln_apply(float x, float m_bf, float rs) { return rbf(rbf(x - m_bf) * rs); }

constexpr int BM = 64;
constexpr int AS_RES = D_ + 8;
constexpr int AS_STR = 32 + 8;

template <int EPI> struct Cfg;
template <> struct Cfg<0> { static constexpr int BN = 240, PADB = 0, K = 640; };
template <> struct Cfg<1> { static constexpr int BN = 80, PADB = 0, K = 640; };
template <> struct Cfg<2> { static constexpr int BN = 192, PADB = 16, K = 640; };

__device__ __forceinline__ int qkv_col(int hg, int c) {
  int part = c / 80; return part * 640 + hg * 80 + (c - part * 80);
}

template <int EPI, int BN, int PADB, int KDIM>
__global__ void __launch_bounds__(256)
gemm_nn(const u16* __restrict__ A, const u16* __restrict__ B, int ldb,
        const u16* __restrict__ res, u16* __restrict__ out0, u16* __restrict__ out1,
        const u16* __restrict__ wphi, float* __restrict__ kvpart) {
  constexpr bool ARES = (EPI != 1);
  constexpr int BK = 32;
  constexpr int BSTR = BN + PADB;
  constexpr int NT = BN / 16;
  constexpr int NTW = (NT + 1) / 2;
  constexpr int KSTEPS = KDIM / BK;
  constexpr int A_ELEMS = ARES ? BM * AS_RES : 2 * BM * AS_STR;
  constexpr int B_ELEMS = 2 * BK * BSTR;
  constexpr int CSTR = BN + 8;
  constexpr int EPI0_ELEMS = BM * CSTR + BM * 4 * 24 + BM * (160 + 8);
  constexpr int MAIN_ELEMS = A_ELEMS + B_ELEMS;
  constexpr int SMEM_ELEMS = (EPI == 0 && EPI0_ELEMS > MAIN_ELEMS) ? EPI0_ELEMS : MAIN_ELEMS;
  __shared__ __attribute__((aligned(16))) u16 smem[SMEM_ELEMS];
  u16* As = smem;
  u16* Bs = smem + A_ELEMS;

  const int tid = threadIdx.x, lane = tid & 63, w = tid >> 6;
  const int wm = w & 1, wn = w >> 1;
  const int NCT = (EPI == 0) ? 8 : (EPI == 1 ? 8 : 8);
  const int mt_blk = blockIdx.x / NCT, ct = blockIdx.x % NCT;
  const int row0 = mt_blk * BM;
  const int g = lane >> 4, li = lane & 15, lq = li >> 2, lp = li & 3;

  constexpr int BCH = BK * (BN / 8);
  constexpr int BLD = (BCH + 255) / 256;
  u32x4 breg[BLD];
  auto bload = [&](int kt) {
#pragma unroll
    for (int i = 0; i < BLD; ++i) {
      int c = tid + i * 256;
      if (BCH % 256 == 0 || c < BCH) {
        int r = c / (BN / 8), cc = c % (BN / 8);
        int col = (EPI == 0) ? qkv_col(ct, cc * 8) : ct * BN + cc * 8;
        breg[i] = *(const u32x4*)(B + (size_t)(kt * BK + r) * ldb + col);
      }
    }
  };
  auto bstore = [&](int buf) {
#pragma unroll
    for (int i = 0; i < BLD; ++i) {
      int c = tid + i * 256;
      if (BCH % 256 == 0 || c < BCH) {
        int r = c / (BN / 8), cc = c % (BN / 8);
        *(u32x4*)(Bs + buf * BK * BSTR + r * BSTR + cc * 8) = breg[i];
      }
    }
  };
  u32x4 areg;
  auto aload = [&](int kt) {
    int r = tid >> 2, cc = tid & 3;
    areg = *(const u32x4*)(A + (size_t)(row0 + r) * KDIM + kt * BK + cc * 8);
  };
  auto astore = [&](int buf) {
    int r = tid >> 2, cc = tid & 3;
    u16* dst = As + buf * BM * AS_STR + r * AS_STR;
    u32x2 lo = {areg[0], areg[1]}, hi = {areg[2], areg[3]};
    *(u32x2*)(dst + 8 * ((2 * cc) & 3) + 4 * (cc >> 1)) = lo;
    *(u32x2*)(dst + 8 * ((2 * cc + 1) & 3) + 4 * (cc >> 1)) = hi;
  };

  bload(0);
  if constexpr (!ARES) aload(0);

  if constexpr (ARES) {
    const int r = tid >> 2, part = tid & 3;
    u32x4 xv[20];
    const u16* xr = A + (size_t)(row0 + r) * D_;
#pragma unroll
    for (int i = 0; i < 20; ++i) xv[i] = *(const u32x4*)(xr + (part + 4 * i) * 8);
    float s = 0.f;
#pragma unroll
    for (int i = 0; i < 20; ++i)
#pragma unroll
      for (int j = 0; j < 4; ++j) { s += bf2f(xv[i][j] & 0xffff); s += bf2f(xv[i][j] >> 16); }
    s += __shfl_xor(s, 1); s += __shfl_xor(s, 2);
    const float mean = s * (1.0f / D_);
    float v = 0.f;
#pragma unroll
    for (int i = 0; i < 20; ++i)
#pragma unroll
      for (int j = 0; j < 4; ++j) {
        float a = bf2f(xv[i][j] & 0xffff) - mean, b = bf2f(xv[i][j] >> 16) - mean;
        v += a * a; v += b * b;
      }
    v += __shfl_xor(v, 1); v += __shfl_xor(v, 2);
    const float m_bf = rbf(mean);
    const float var_bf = rbf(v * (1.0f / D_));
    const float sd = rbf(sqrtf(rbf(var_bf + 1e-5f)));
    const float rs = 1.0f / sd;
#pragma unroll
    for (int i = 0; i < 20; ++i) {
      int c = part + 4 * i;
      unsigned o[4];
#pragma unroll
      for (int j = 0; j < 4; ++j) {
        u16 lo = f2bf(ln_apply(bf2f(xv[i][j] & 0xffff), m_bf, rs));
        u16 hi = f2bf(ln_apply(bf2f(xv[i][j] >> 16), m_bf, rs));
        o[j] = (unsigned)lo | ((unsigned)hi << 16);
      }
      int blk = c >> 2, cc = c & 3;
      u16* dst = As + r * AS_RES + blk * 32;
      u32x2 lo2 = {o[0], o[1]}, hi2 = {o[2], o[3]};
      *(u32x2*)(dst + 8 * ((2 * cc) & 3) + 4 * (cc >> 1)) = lo2;
      *(u32x2*)(dst + 8 * ((2 * cc + 1) & 3) + 4 * (cc >> 1)) = hi2;
    }
  }

  bstore(0);
  if constexpr (!ARES) astore(0);
  if (KSTEPS > 1) { bload(1); if constexpr (!ARES) aload(1); }
  __syncthreads();

  f32x4 acc[2][NTW];
#pragma unroll
  for (int a = 0; a < 2; ++a)
#pragma unroll
    for (int b = 0; b < NTW; ++b) acc[a][b] = f32x4{0.f, 0.f, 0.f, 0.f};
  const int nt0 = wn * NTW;
  const int ntw = (NT - nt0) < NTW ? (NT - nt0) : NTW;

  for (int kt = 0; kt < KSTEPS; ++kt) {
    const int buf = kt & 1;
    bf16x8 af[2];
#pragma unroll
    for (int mt = 0; mt < 2; ++mt) {
      int r = wm * 32 + mt * 16 + li;
      const u16* p = ARES ? (As + r * AS_RES + kt * BK + 8 * g) : (As + buf * BM * AS_STR + r * AS_STR + 8 * g);
      af[mt] = *(const bf16x8*)p;
    }
    const u16* bb = Bs + buf * BK * BSTR + (4 * g + lq) * BSTR + 4 * lp;
#pragma unroll
    for (int nt = 0; nt < NTW; ++nt) {
      if (nt < ntw) {
        int n0 = (nt0 + nt) * 16;
        s16x4 b0 = trread(bb + n0);
        s16x4 b1 = trread(bb + 16 * BSTR + n0);
        bf16x8 bf = cat4(b0, b1);
        acc[0][nt] = mfma16(af[0], bf, acc[0][nt]);
        acc[1][nt] = mfma16(af[1], bf, acc[1][nt]);
      }
    }
    if (kt + 1 < KSTEPS) {
      bstore(buf ^ 1);
      if constexpr (!ARES) astore(buf ^ 1);
      if (kt + 2 < KSTEPS) { bload(kt + 2); if constexpr (!ARES) aload(kt + 2); }
    }
    __syncthreads();
  }

  if constexpr (EPI == 1) {
#pragma unroll
    for (int mt = 0; mt < 2; ++mt)
#pragma unroll
      for (int nt = 0; nt < NTW; ++nt)
        if (nt < ntw) {
#pragma unroll
          for (int i = 0; i < 4; ++i) {
            int r = row0 + wm * 32 + mt * 16 + 4 * g + i;
            int c = ct * BN + (nt0 + nt) * 16 + li;
            size_t idx = (size_t)r * D_ + c;
            out0[idx] = f2bf(bf2f(res[idx]) + rbf(acc[mt][nt][i]));
          }
        }
  } else if constexpr (EPI == 2) {
#pragma unroll
    for (int mt = 0; mt < 2; ++mt)
#pragma unroll
      for (int nt = 0; nt < NTW; ++nt)
        if (nt < ntw) {
#pragma unroll
          for (int i = 0; i < 4; ++i) {
            int r = row0 + wm * 32 + mt * 16 + 4 * g + i;
            int c = ct * BN + (nt0 + nt) * 16 + li;
            float x = rbf(acc[mt][nt][i]);
            float u = 0.7978845608028654f * (x + 0.044715f * x * x * x);
            float t = 1.0f - 2.0f / (__expf(2.0f * u) + 1.0f);
            u16 z = f2bf(0.5f * x * (1.0f + t));
            if (c < FH_) out0[(size_t)r * FH_ + c] = z; else out1[(size_t)r * FH_ + (c - FH_)] = z;
          }
        }
  } else {
    u16* Cs = smem;
    u16* PK = smem + BM * CSTR;
    u16* PHI = PK + BM * 4 * 24;
    constexpr int PHS = 168;
#pragma unroll
    for (int mt = 0; mt < 2; ++mt)
#pragma unroll
      for (int nt = 0; nt < NTW; ++nt)
        if (nt < ntw) {
#pragma unroll
          for (int i = 0; i < 4; ++i) {
            int r = wm * 32 + mt * 16 + 4 * g + i;
            int c = (nt0 + nt) * 16 + li;
            Cs[r * CSTR + c] = f2bf(acc[mt][nt][i]);
          }
        }
    __syncthreads();
    for (int i = tid; i < BM * 10; i += 256) {
      int r = i / 10, c = (i % 10) * 8;
      u32x4 v = *(const u32x4*)(Cs + r * CSTR + c);
      *(u32x4*)(out0 + (size_t)(row0 + r) * D_ + ct * 80 + c) = v;
    }
    {
      const int h = ct * 4 + w;
      bf16x8 wb[2];
#pragma unroll
      for (int n2 = 0; n2 < 2; ++n2) {
        int n = n2 * 16 + li;
#pragma unroll
        for (int j = 0; j < 8; ++j) {
          int k = 8 * g + j;
          u16 v = 0;
          if (k < DH_ && n < DH_) v = wphi[(size_t)h * DH_ * DH_ + k * DH_ + n];
          wb[n2][j] = (short)v;
        }
      }
#pragma unroll
      for (int mt = 0; mt < 4; ++mt) {
        const u16* ap = Cs + (mt * 16 + li) * CSTR + 80 + 20 * w + 8 * g;
        u32x2 a0 = *(const u32x2*)ap, a1 = *(const u32x2*)(ap + 4);
        u32x4 av = {a0[0], a0[1], a1[0], a1[1]};
        bf16x8 af = __builtin_bit_cast(bf16x8, av);
#pragma unroll
        for (int n2 = 0; n2 < 2; ++n2) {
          f32x4 p = mfma16(af, wb[n2], f32x4{0.f, 0.f, 0.f, 0.f});
          int n = n2 * 16 + li;
          if (n < DH_) {
#pragma unroll
            for (int i = 0; i < 4; ++i) PK[((mt * 16 + 4 * g + i) * 4 + w) * 24 + n] = f2bf(p[i]);
          }
        }
      }
    }
    __syncthreads();
    {
      const int r = tid >> 2, hh = tid & 3;
      const u16* pp = PK + (r * 4 + hh) * 24;
      float p[20];
#pragma unroll
      for (int j = 0; j < 20; ++j) p[j] = bf2f(pp[j]);
      float mx = p[0], mn = p[0];
#pragma unroll
      for (int j = 1; j < 20; ++j) { mx = fmaxf(mx, p[j]); mn = fminf(mn, p[j]); }
      float e1[20], e2[20], s1 = 0.f, s2 = 0.f;
#pragma unroll
      for (int j = 0; j < 20; ++j) {
        e1[j] = __expf(p[j] - mx); s1 += e1[j];
        e2[j] = __expf(mn - p[j]); s2 += e2[j];
      }
      float r1 = 1.0f / s1, r2 = 1.0f / s2;
      u16* dst = PHI + r * PHS + hh * 40;
#pragma unroll
      for (int j = 0; j < 20; ++j) { dst[j] = f2bf(e1[j] * r1); dst[20 + j] = f2bf(e2[j] * r2); }
    }
    __syncthreads();
    {
      const int h = ct * 4 + w;
      f32x4 kv[3][2];
#pragma unroll
      for (int a = 0; a < 3; ++a)
#pragma unroll
        for (int b = 0; b < 2; ++b) kv[a][b] = f32x4{0.f, 0.f, 0.f, 0.f};
#pragma unroll
      for (int ks = 0; ks < 2; ++ks) {
        bf16x8 vb[2];
#pragma unroll
        for (int n2 = 0; n2 < 2; ++n2) {
          const u16* bp = Cs + (ks * 32 + 8 * g + lq) * CSTR + 160 + 20 * w + n2 * 16 + 4 * lp;
          s16x4 b0 = trread(bp), b1 = trread(bp + 4 * CSTR);
          bf16x8 bf = cat4(b0, b1);
          if (n2 == 1) {
            short fill = (li == 4) ? (short)0x3F80 : (short)0;
            if (li >= 4) {
#pragma unroll
              for (int j = 0; j < 8; ++j) bf[j] = fill;
            }
          }
          vb[n2] = bf;
        }
#pragma unroll
        for (int mt = 0; mt < 3; ++mt) {
          const u16* ap = PHI + (ks * 32 + 8 * g + lq) * PHS + 40 * w + mt * 16 + 4 * lp;
          s16x4 a0 = trread(ap), a1 = trread(ap + 4 * PHS);
          bf16x8 af = cat4(a0, a1);
#pragma unroll
          for (int n2 = 0; n2 < 2; ++n2) kv[mt][n2] = mfma16(af, vb[n2], kv[mt][n2]);
        }
      }
      float* dst = kvpart + ((size_t)mt_blk * H_ + h) * 960;
#pragma unroll
      for (int mt = 0; mt < 3; ++mt)
#pragma unroll
        for (int n2 = 0; n2 < 2; ++n2) {
          int d = n2 * 16 + li;
          if (d < 21) {
#pragma unroll
            for (int i = 0; i < 4; ++i) {
              int f = mt * 16 + 4 * g + i;
              if (f < 40) dst[f * 24 + d] = kv[mt][n2][i];
            }
          }
        }
    }
  }
}

__global__ void __launch_bounds__(256)
attn_apply(const u16* __restrict__ q, const u16* __restrict__ wphi, const float* __restrict__ kvpart,
           u16* __restrict__ o) {
  __shared__ __attribute__((aligned(16))) u16 KVZ[64 * 32];
  __shared__ __attribute__((aligned(16))) u16 PQ[256 * 24];
  __shared__ __attribute__((aligned(16))) u16 PHI[256 * 72];
  const int tid = threadIdx.x, lane = tid & 63, w = tid >> 6;
  const int h = blockIdx.x & 31, rb = blockIdx.x >> 5;
  const int g = lane >> 4, li = lane & 15;
  const int row0 = rb * 256;
  for (int i = tid; i < 64 * 32; i += 256) KVZ[i] = 0;
  __syncthreads();
  for (int idx = tid; idx < 960; idx += 256) {
    int f = idx / 24, d = idx % 24;
    if (d < 21) {
      float s = 0.f;
      for (int rt = 0; rt < 32; ++rt) s += kvpart[((size_t)rt * H_ + h) * 960 + idx];
      KVZ[f * 32 + d] = f2bf(s);
    }
  }
  {
    bf16x8 wb[2];
#pragma unroll
    for (int n2 = 0; n2 < 2; ++n2) {
      int n = n2 * 16 + li;
#pragma unroll
      for (int j = 0; j < 8; ++j) {
        int k = 8 * g + j;
        u16 v = 0;
        if (k < DH_ && n < DH_) v = wphi[(size_t)h * DH_ * DH_ + k * DH_ + n];
        wb[n2][j] = (short)v;
      }
    }
#pragma unroll
    for (int mt = 0; mt < 4; ++mt) {
      int r = w * 64 + mt * 16 + li;
      const u16* ap = q + (size_t)(row0 + r) * D_ + h * DH_ + 8 * g;
      u32x4 av = {0u, 0u, 0u, 0u};
      if (g < 2) { u32x2 a0 = *(const u32x2*)ap, a1 = *(const u32x2*)(ap + 4); av = u32x4{a0[0], a0[1], a1[0], a1[1]}; }
      else if (g == 2) { u32x2 a0 = *(const u32x2*)ap; av[0] = a0[0]; av[1] = a0[1]; }
      bf16x8 af = __builtin_bit_cast(bf16x8, av);
#pragma unroll
      for (int n2 = 0; n2 < 2; ++n2) {
        f32x4 p = mfma16(af, wb[n2], f32x4{0.f, 0.f, 0.f, 0.f});
        int n = n2 * 16 + li;
        if (n < DH_) {
#pragma unroll
          for (int i = 0; i < 4; ++i) PQ[(w * 64 + mt * 16 + 4 * g + i) * 24 + n] = f2bf(p[i]);
        }
      }
    }
  }
  __syncthreads();
  {
    const int r = tid;
    const u16* pp = PQ + r * 24;
    float p[20];
#pragma unroll
    for (int j = 0; j < 20; ++j) p[j] = bf2f(pp[j]);
    float mx = p[0], mn = p[0];
#pragma unroll
    for (int j = 1; j < 20; ++j) { mx = fmaxf(mx, p[j]); mn = fminf(mn, p[j]); }
    float e1[20], e2[20], s1 = 0.f, s2 = 0.f;
#pragma unroll
    for (int j = 0; j < 20; ++j) {
      e1[j] = __expf(p[j] - mx); s1 += e1[j];
      e2[j] = __expf(mn - p[j]); s2 += e2[j];
    }
    float r1 = 1.0f / s1, r2 = 1.0f / s2;
    u16* dst = PHI + r * 72;
#pragma unroll
    for (int j = 0; j < 20; ++j) { dst[j] = f2bf(e1[j] * r1); dst[20 + j] = f2bf(e2[j] * r2); }
#pragma unroll
    for (int j = 40; j < 64; ++j) dst[j] = 0;
  }
  __syncthreads();
  {
    bf16x8 kb[2][2];
#pragma unroll
    for (int ks = 0; ks < 2; ++ks)
#pragma unroll
      for (int n2 = 0; n2 < 2; ++n2)
#pragma unroll
        for (int j = 0; j < 8; ++j) kb[ks][n2][j] = (short)KVZ[(ks * 32 + 8 * g + j) * 32 + n2 * 16 + li];
#pragma unroll
    for (int mt = 0; mt < 4; ++mt) {
      int r = w * 64 + mt * 16 + li;
      f32x4 num0 = {0.f, 0.f, 0.f, 0.f}, num1 = {0.f, 0.f, 0.f, 0.f};
#pragma unroll
      for (int ks = 0; ks < 2; ++ks) {
        bf16x8 af = *(const bf16x8*)(PHI + r * 72 + ks * 32 + 8 * g);
        num0 = mfma16(af, kb[ks][0], num0);
        num1 = mfma16(af, kb[ks][1], num1);
      }
#pragma unroll
      for (int i = 0; i < 4; ++i) {
        float den = __shfl(num1[i], g * 16 + 4);
        den = rbf(rbf(den) + 1e-6f);
        int rr = row0 + w * 64 + mt * 16 + 4 * g + i;
        o[(size_t)rr * D_ + h * DH_ + li] = f2bf(rbf(num0[i]) / den);
        if (li < 4) o[(size_t)rr * D_ + h * DH_ + 16 + li] = f2bf(rbf(num1[i]) / den);
      }
    }
  }
}

__global__ void __launch_bounds__(256)
ln_z2_t(const u16* __restrict__ z2raw, u16* __restrict__ z2nT) {
  __shared__ float stat[64][2];
  __shared__ __attribute__((aligned(16))) u16 T[96 * 72];
  const int tid = threadIdx.x;
  const int rb = blockIdx.x >> 3, cs = blockIdx.x & 7;
  const int row0 = rb * 64;
  {
    const int r = tid >> 2, part = tid & 3;
    const u16* zr = z2raw + (size_t)(row0 + r) * FH_;
    u32x4 xv[24];
#pragma unroll
    for (int i = 0; i < 24; ++i) xv[i] = *(const u32x4*)(zr + (part + 4 * i) * 8);
    float s = 0.f;
#pragma unroll
    for (int i = 0; i < 24; ++i)
#pragma unroll
      for (int j = 0; j < 4; ++j) { s += bf2f(xv[i][j] & 0xffff); s += bf2f(xv[i][j] >> 16); }
    s += __shfl_xor(s, 1); s += __shfl_xor(s, 2);
    const float mean = s * (1.0f / FH_);
    float v = 0.f;
#pragma unroll
    for (int i = 0; i < 24; ++i)
#pragma unroll
      for (int j = 0; j < 4; ++j) {
        float a = bf2f(xv[i][j] & 0xffff) - mean, b = bf2f(xv[i][j] >> 16) - mean;
        v += a * a; v += b * b;
      }
    v += __shfl_xor(v, 1); v += __shfl_xor(v, 2);
    if (part == 0) {
      stat[r][0] = rbf(mean);
      stat[r][1] = 1.0f / rbf(sqrtf(rbf(rbf(v * (1.0f / FH_)) + 1e-5f)));
    }
  }
  __syncthreads();
  for (int i = tid; i < 64 * 12; i += 256) {
    int r = i / 12, c = (i % 12) * 8;
    u32x4 xv = *(const u32x4*)(z2raw + (size_t)(row0 + r) * FH_ + cs * 96 + c);
    float m = stat[r][0], rs = stat[r][1];
#pragma unroll
    for (int j = 0; j < 4; ++j) {
      T[(c + 2 * j) * 72 + r] = f2bf(ln_apply(bf2f(xv[j] & 0xffff), m, rs));
      T[(c + 2 * j + 1) * 72 + r] = f2bf(ln_apply(bf2f(xv[j] >> 16), m, rs));
    }
  }
  __syncthreads();
  for (int i = tid; i < 96 * 8; i += 256) {
    int f = i >> 3, c = (i & 7) * 8;
    u32x4 v = *(const u32x4*)(T + f * 72 + c);
    *(u32x4*)(z2nT + (size_t)(cs * 96 + f) * S_ + row0 + c) = v;
  }
}

__global__ void __launch_bounds__(256)
gemm_sp(const u16* __restrict__ wsp, const u16* __restrict__ z2nT, const u16* __restrict__ bsp,
        const u16* __restrict__ z1, u16* __restrict__ hout) {
  constexpr int BN = 96, BK = 64, STR = BK + 8;
  __shared__ __attribute__((aligned(16))) u16 As[2][BM * STR];
  __shared__ __attribute__((aligned(16))) u16 Bs[2][BN * STR];
  const int tid = threadIdx.x, lane = tid & 63, w = tid >> 6;
  const int wm = w & 1, wn = w >> 1;
  const int mb = blockIdx.x >> 3, nb = blockIdx.x & 7;
  const int row0 = mb * BM, col0 = nb * BN;
  const int g = lane >> 4, li = lane & 15;
  u32x4 ar[2], br[3];
  auto gload = [&](int kt) {
#pragma unroll
    for (int i = 0; i < 2; ++i) {
      int c = tid + i * 256; int r = c >> 3, cc = c & 7;
      ar[i] = *(const u32x4*)(wsp + (size_t)(row0 + r) * S_ + kt * BK + cc * 8);
    }
#pragma unroll
    for (int i = 0; i < 3; ++i) {
      int c = tid + i * 256; int r = c >> 3, cc = c & 7;
      br[i] = *(const u32x4*)(z2nT + (size_t)(col0 + r) * S_ + kt * BK + cc * 8);
    }
  };
  auto sstore = [&](int buf) {
#pragma unroll
    for (int i = 0; i < 2; ++i) { int c = tid + i * 256; int r = c >> 3, cc = c & 7; *(u32x4*)(&As[buf][r * STR + cc * 8]) = ar[i]; }
#pragma unroll
    for (int i = 0; i < 3; ++i) { int c = tid + i * 256; int r = c >> 3, cc = c & 7; *(u32x4*)(&Bs[buf][r * STR + cc * 8]) = br[i]; }
  };
  constexpr int KSTEPS = S_ / BK;
  gload(0); sstore(0); gload(1);
  __syncthreads();
  f32x4 acc[2][3];
#pragma unroll
  for (int a = 0; a < 2; ++a)
#pragma unroll
    for (int b = 0; b < 3; ++b) acc[a][b] = f32x4{0.f, 0.f, 0.f, 0.f};
  for (int kt = 0; kt < KSTEPS; ++kt) {
    const int buf = kt & 1;
#pragma unroll
    for (int ks = 0; ks < 2; ++ks) {
      bf16x8 af[2], bf[3];
#pragma unroll
      for (int mt = 0; mt < 2; ++mt) af[mt] = *(const bf16x8*)(&As[buf][(wm * 32 + mt * 16 + li) * STR + ks * 32 + 8 * g]);
#pragma unroll
      for (int nt = 0; nt < 3; ++nt) bf[nt] = *(const bf16x8*)(&Bs[buf][(wn * 48 + nt * 16 + li) * STR + ks * 32 + 8 * g]);
#pragma unroll
      for (int mt = 0; mt < 2; ++mt)
#pragma unroll
        for (int nt = 0; nt < 3; ++nt) acc[mt][nt] = mfma16(af[mt], bf[nt], acc[mt][nt]);
    }
    if (kt + 1 < KSTEPS) { sstore(buf ^ 1); if (kt + 2 < KSTEPS) gload(kt + 2); }
    __syncthreads();
  }
#pragma unroll
  for (int mt = 0; mt < 2; ++mt)
#pragma unroll
    for (int nt = 0; nt < 3; ++nt)
#pragma unroll
      for (int i = 0; i < 4; ++i) {
        int r = row0 + wm * 32 + mt * 16 + 4 * g + i;
        int c = col0 + wn * 48 + nt * 16 + li;
        float gg = rbf(rbf(acc[mt][nt][i]) + bf2f(bsp[r]));
        size_t idx = (size_t)r * FH_ + c;
        hout[idx] = f2bf(bf2f(z1[idx]) * gg);
      }
}

extern "C" void kernel_launch(void* const* d_in, const int* in_sizes, int n_in,
                              void* d_out, int out_size, void* d_ws, size_t ws_size,
                              hipStream_t stream) {
  const u16* A_x = (const u16*)d_in[0];
  const u16* wqkv = (const u16*)d_in[1];
  const u16* wphi = (const u16*)d_in[2];
  const u16* wo = (const u16*)d_in[3];
  const u16* w_in = (const u16*)d_in[5];
  const u16* w_sp = (const u16*)d_in[6];
  const u16* b_sp = (const u16*)d_in[7];
  const u16* w_out = (const u16*)d_in[8];
  u16* out = (u16*)d_out;

  char* ws = (char*)d_ws;
  size_t off = 0;
  auto alloc = [&](size_t bytes) { char* p = ws + off; off += (bytes + 255) & ~(size_t)255; return p; };
  u16* xa = (u16*)alloc((size_t)S_ * D_ * 2);
  u16* xb = (u16*)alloc((size_t)S_ * D_ * 2);
  u16* qb = (u16*)alloc((size_t)S_ * D_ * 2);
  u16* ob = (u16*)alloc((size_t)S_ * D_ * 2);
  float* kvp = (float*)alloc((size_t)32 * H_ * 960 * 4);
  u16* z1 = (u16*)alloc((size_t)S_ * FH_ * 2);
  u16* z2r = (u16*)alloc((size_t)S_ * FH_ * 2);
  u16* z2t = (u16*)alloc((size_t)S_ * FH_ * 2);
  u16* hb = (u16*)alloc((size_t)S_ * FH_ * 2);

  const u16* xin = A_x;
  for (int rep = 0; rep < 2; ++rep) {
    u16* x1 = xa;
    u16* x2 = (rep == 0) ? xb : out;
    gemm_nn<0, 240, 0, 640><<<256, 256, 0, stream>>>(xin, wqkv, 3 * D_, nullptr, qb, nullptr, wphi, kvp);
    attn_apply<<<256, 256, 0, stream>>>(qb, wphi, kvp, ob);
    gemm_nn<1, 80, 0, 640><<<256, 256, 0, stream>>>(ob, wo, D_, xin, x1, nullptr, nullptr, nullptr);
    gemm_nn<2, 192, 16, 640><<<256, 256, 0, stream>>>(x1, w_in, 2 * FH_, nullptr, z1, z2r, nullptr, nullptr);
    ln_z2_t<<<256, 256, 0, stream>>>(z2r, z2t);
    gemm_sp<<<256, 256, 0, stream>>>(w_sp, z2t, b_sp, z1, hb);
    gemm_nn<1, 80, 0, 768><<<256, 256, 0, stream>>>(hb, w_out, D_, x1, x2, nullptr, nullptr, nullptr);
    xin = x2;
  }
}
```

```cpp
#include <hip/hip_runtime.h>
#include <hip/hip_bf16.h>
#include <stdint.h>

typedef unsigned short u16;
typedef short bf16x8 __attribute__((ext_vector_type(8)));
typedef short s16x4 __attribute__((ext_vector_type(4)));
typedef float f32x4 __attribute__((ext_vector_type(4)));
typedef unsigned int u32x4 __attribute__((ext_vector_type(4)));
typedef unsigned int u32x2 __attribute__((ext_vector_type(2)));
#define LDS3(T, p) ((__attribute__((address_space(3))) T*)(p))

constexpr int S_ = 2048, D_ = 640, H_ = 32, DH_ = 20, FH_ = 768;

__device__ __forceinline__ float bf2f(u16 v) { return __uint_as_float(((unsigned)v) << 16); }
__device__ __forceinline__ u16 f2bf(float f) { return __builtin_bit_cast(u16, (__bf16)f); }
__device__ __forceinline__ float rbf(float f) { return bf2f(f2bf(f)); }
__device__ __forceinline__ f32x4 mfma16(bf16x8 a, bf16x8 b, f32x4 c) {
  return __builtin_amdgcn_mfma_f32_16x16x32_bf16(a, b, c, 0, 0, 0);
}
__device__ __forceinline__ s16x4 trread(const u16* lds_ptr) {
  return __builtin_amdgcn_ds_read_tr16_b64_v4i16(LDS3(s16x4, lds_ptr));
}
__device__ __forceinline__ bf16x8 cat4(s16x4 a, s16x4 b) {
  bf16x8 r; r[0]=a[0]; r[1]=a[1]; r[2]=a[2]; r[3]=a[3]; r[4]=b[0]; r[5]=b[1]; r[6]=b[2]; r[7]=b[3]; return r;
}

__device__ __forceinline__ float ln_apply(float x, float m_bf, float rs) { return rbf(rbf(x - m_bf) * rs); }

constexpr int BM = 64;
#define WAIT_VM(N) asm volatile("s_waitcnt vmcnt(%0)" :: "n"(N) : "memory")
#define WAIT_LGKM0() asm volatile("s_waitcnt lgkmcnt(0)" ::: "memory")
#define RAW_BARRIER() do { WAIT_LGKM0(); __builtin_amdgcn_s_barrier(); } while (0)
__device__ __forceinline__ void dma16(const u16* src, char* lds_dst) {
  __builtin_amdgcn_global_load_lds((const unsigned*)src, (unsigned*)lds_dst, 16, 0, 0);
}

template <int EPI, int BN, int SW, int KDIM, int BK, int NST>
__global__ void __launch_bounds__(256)
gemm_nn(const u16* __restrict__ A, const u16* __restrict__ B, int ldb,
        const u16* __restrict__ res, u16* __restrict__ out0, u16* __restrict__ out1,
        const u16* __restrict__ wphi, float* __restrict__ kvpart) {
  constexpr bool ARES = (EPI != 1);
  constexpr int NT = BN / 16, NTW = (NT + 1) / 2, NTS = SW / 16;
  constexpr int KSTEPS = KDIM / BK;
  constexpr int ACH = ARES ? 0 : BM * BK / 8;
  constexpr int BCH = BK * BN / 8;
  constexpr int NI = (ACH + BCH + 255) / 256;
  constexpr int STAGE_B = NI * 256 * 16;
  constexpr int ARES_B = ARES ? BM * KDIM * 2 : 0;
  constexpr int CSTR = BN + 8;
  constexpr int EPI0_B = (BM * CSTR + BM * 4 * 24 + BM * 168) * 2;
  constexpr int MAIN_B = ARES_B + NST * STAGE_B;
  constexpr int SMEM_B = (EPI == 0 && EPI0_B > MAIN_B) ? EPI0_B : MAIN_B;
  static_assert(ACH % 256 == 0, "A chunks must fill whole instructions");
  __shared__ __attribute__((aligned(16))) char smem[SMEM_B];
  char* stg = smem + ARES_B;

  const int tid = threadIdx.x, lane = tid & 63, w = tid >> 6;
  const int wm = w & 1, wn = w >> 1;
  constexpr int NCT = 8;
  const int mt_blk = blockIdx.x / NCT, ct = blockIdx.x % NCT;
  const int row0 = mt_blk * BM;
  const int g = lane >> 4, li = lane & 15, lq = li >> 2, lp = li & 3;

  const u16* src[NI];
  int inc[NI];
#pragma unroll
  for (int i = 0; i < NI; ++i) {
    int c = i * 256 + tid;
    if (i < ACH / 256) {
      int r = c >> 3, pc = c & 7;
      src[i] = A + (size_t)(row0 + r) * KDIM + ((pc ^ (r & 7)) * 8);
      inc[i] = BK;
    } else {
      int b = c - ACH;
      if (b < BCH) {
        int sub = b / (BK * SW / 8), rem = b % (BK * SW / 8);
        int prow = rem / (SW / 8), cc = rem % (SW / 8);
        int row = prow ^ (((prow >> 3) & 1) << 2);
        int col = (EPI == 0) ? (sub * 640 + ct * 80 + cc * 8) : (ct * BN + sub * SW + cc * 8);
        src[i] = B + (size_t)row * ldb + col;
        inc[i] = BK * ldb;
      } else { src[i] = B; inc[i] = 0; }
    }
  }
  auto issue = [&](int kt, int buf) {
#pragma unroll
    for (int i = 0; i < NI; ++i) dma16(src[i] + (size_t)kt * inc[i], stg + buf * STAGE_B + (i * 256 + tid) * 16);
  };
#pragma unroll
  for (int s = 0; s < NST - 1; ++s) issue(s, s);

  if constexpr (ARES) {
    const int r = tid >> 2, part = tid & 3;
    u32x4 xv[20];
    const u16* xr = A + (size_t)(row0 + r) * D_;
#pragma unroll
    for (int i = 0; i < 20; ++i) xv[i] = *(const u32x4*)(xr + (part + 4 * i) * 8);
    float s = 0.f;
#pragma unroll
    for (int i = 0; i < 20; ++i)
#pragma unroll
      for (int j = 0; j < 4; ++j) { s += bf2f(xv[i][j] & 0xffff); s += bf2f(xv[i][j] >> 16); }
    s += __shfl_xor(s, 1); s += __shfl_xor(s, 2);
    const float mean = s * (1.0f / D_);
    float v = 0.f;
#pragma unroll
    for (int i = 0; i < 20; ++i)
#pragma unroll
      for (int j = 0; j < 4; ++j) {
        float a = bf2f(xv[i][j] & 0xffff) - mean, b = bf2f(xv[i][j] >> 16) - mean;
        v += a * a; v += b * b;
      }
    v += __shfl_xor(v, 1); v += __shfl_xor(v, 2);
    const float m_bf = rbf(mean);
    const float sd = rbf(sqrtf(rbf(rbf(v * (1.0f / D_)) + 1e-5f)));
    const float rs = 1.0f / sd;
#pragma unroll
    for (int i = 0; i < 20; ++i) {
      int c = part + 4 * i;
      u32x4 o;
#pragma unroll
      for (int j = 0; j < 4; ++j) {
        u16 lo = f2bf(ln_apply(bf2f(xv[i][j] & 0xffff), m_bf, rs));
        u16 hi = f2bf(ln_apply(bf2f(xv[i][j] >> 16), m_bf, rs));
        o[j] = (unsigned)lo | ((unsigned)hi << 16);
      }
      int pc = (c & ~7) | ((c ^ r) & 7);
      *(u32x4*)(smem + r * (KDIM * 2) + pc * 16) = o;
    }
  }

  f32x4 acc[2][NTW];
#pragma unroll
  for (int a = 0; a < 2; ++a)
#pragma unroll
    for (int b = 0; b < NTW; ++b) acc[a][b] = f32x4{0.f, 0.f, 0.f, 0.f};
  const int nt0 = wn * NTW;
  const int ntw = (NT - nt0) < NTW ? (NT - nt0) : NTW;

  int arow[2];
#pragma unroll
  for (int mt = 0; mt < 2; ++mt) arow[mt] = wm * 32 + mt * 16 + li;
  const int bt0 = (8 * g + 4 * (g & 1) + lq) * (SW * 2) + 8 * lp;
  const int bt1 = (8 * g + 4 * (1 ^ (g & 1)) + lq) * (SW * 2) + 8 * lp;

  int buf = 0;
  for (int kt = 0; kt < KSTEPS; ++kt) {
    if (kt <= KSTEPS - NST + 1) WAIT_VM((NST - 2) * NI); else WAIT_VM(0);
    RAW_BARRIER();
    if (kt + NST - 1 < KSTEPS) { int nb = buf + NST - 1; if (nb >= NST) nb -= NST; issue(kt + NST - 1, nb); }
    const char* sb = stg + buf * STAGE_B;
#pragma unroll
    for (int ks = 0; ks < BK / 32; ++ks) {
      bf16x8 af[2];
#pragma unroll
      for (int mt = 0; mt < 2; ++mt) {
        int r = arow[mt];
        if constexpr (ARES) {
          int c = kt * (BK / 8) + ks * 4 + g;
          int pc = (c & ~7) | ((c ^ r) & 7);
          af[mt] = *(const bf16x8*)(smem + r * (KDIM * 2) + pc * 16);
        } else {
          int pc = (ks * 4 + g) ^ (r & 7);
          af[mt] = *(const bf16x8*)(sb + r * (BK * 2) + pc * 16);
        }
      }
      const char* bb = sb + ACH * 16 + ks * 32 * (SW * 2);
#pragma unroll
      for (int nt = 0; nt < NTW; ++nt) {
        if (nt < ntw) {
          int ntg = nt0 + nt;
          int sub = ntg / NTS, n0 = (ntg % NTS) * 16;
          const char* bp = bb + sub * (BK * SW * 2) + n0 * 2;
          s16x4 b0 = trread((const u16*)(bp + bt0));
          s16x4 b1 = trread((const u16*)(bp + bt1));
          bf16x8 bf = cat4(b0, b1);
          acc[0][nt] = mfma16(af[0], bf, acc[0][nt]);
          acc[1][nt] = mfma16(af[1], bf, acc[1][nt]);
        }
      }
    }
    if (++buf == NST) buf = 0;
  }
  __syncthreads();

  if constexpr (EPI == 1) {
#pragma unroll
    for (int mt = 0; mt < 2; ++mt)
#pragma unroll
      for (int nt = 0; nt < NTW; ++nt)
        if (nt < ntw) {
#pragma unroll
          for (int i = 0; i < 4; ++i) {
            int r = row0 + wm * 32 + mt * 16 + 4 * g + i;
            int c = ct * BN + (nt0 + nt) * 16 + li;
            size_t idx = (size_t)r * D_ + c;
            out0[idx] = f2bf(bf2f(res[idx]) + rbf(acc[mt][nt][i]));
          }
        }
  } else if constexpr (EPI == 2) {
#pragma unroll
    for (int mt = 0; mt < 2; ++mt)
#pragma unroll
      for (int nt = 0; nt < NTW; ++nt)
        if (nt < ntw) {
#pragma unroll
          for (int i = 0; i < 4; ++i) {
            int r = row0 + wm * 32 + mt * 16 + 4 * g + i;
            int c = ct * BN + (nt0 + nt) * 16 + li;
            float x = rbf(acc[mt][nt][i]);
            float u = 0.7978845608028654f * (x + 0.044715f * x * x * x);
            float t = 1.0f - 2.0f / (__expf(2.0f * u) + 1.0f);
            u16 z = f2bf(0.5f * x * (1.0f + t));
            if (c < FH_) out0[(size_t)r * FH_ + c] = z; else out1[(size_t)r * FH_ + (c - FH_)] = z;
          }
        }
  } else {
    u16* Cs = (u16*)smem;
    u16* PK = Cs + BM * CSTR;
    u16* PHI = PK + BM * 4 * 24;
    constexpr int PHS = 168;
#pragma unroll
    for (int mt = 0; mt < 2; ++mt)
#pragma unroll
      for (int nt = 0; nt < NTW; ++nt)
        if (nt < ntw) {
#pragma unroll
          for (int i = 0; i < 4; ++i) {
            int r = wm * 32 + mt * 16 + 4 * g + i;
            int c = (nt0 + nt) * 16 + li;
            Cs[r * CSTR + c] = f2bf(acc[mt][nt][i]);
          }
        }
    __syncthreads();
    for (int i = tid; i < BM * 10; i += 256) {
      int r = i / 10, c = (i % 10) * 8;
      u32x4 v = *(const u32x4*)(Cs + r * CSTR + c);
      *(u32x4*)(out0 + (size_t)(row0 + r) * D_ + ct * 80 + c) = v;
    }
    {
      const int h = ct * 4 + w;
      bf16x8 wb[2];
#pragma unroll
      for (int n2 = 0; n2 < 2; ++n2) {
        int n = n2 * 16 + li;
#pragma unroll
        for (int j = 0; j < 8; ++j) {
          int k = 8 * g + j;
          u16 v = 0;
          if (k < DH_ && n < DH_) v = wphi[(size_t)h * DH_ * DH_ + k * DH_ + n];
          wb[n2][j] = (short)v;
        }
      }
#pragma unroll
      for (int mt = 0; mt < 4; ++mt) {
        const u16* ap = Cs + (mt * 16 + li) * CSTR + 80 + 20 * w + 8 * g;
        u32x2 a0 = *(const u32x2*)ap, a1 = *(const u32x2*)(ap + 4);
        u32x4 av = {a0[0], a0[1], a1[0], a1[1]};
        bf16x8 af = __builtin_bit_cast(bf16x8, av);
#pragma unroll
        for (int n2 = 0; n2 < 2; ++n2) {
          f32x4 p = mfma16(af, wb[n2], f32x4{0.f, 0.f, 0.f, 0.f});
          int n = n2 * 16 + li;
          if (n < DH_) {
#pragma unroll
            for (int i = 0; i < 4; ++i) PK[((mt * 16 + 4 * g + i) * 4 + w) * 24 + n] = f2bf(p[i]);
          }
        }
      }
    }
    __syncthreads();
    {
      const int r = tid >> 2, hh = tid & 3;
      const u16* pp = PK + (r * 4 + hh) * 24;
      float p[20];
#pragma unroll
      for (int j = 0; j < 20; ++j) p[j] = bf2f(pp[j]);
      float mx = p[0], mn = p[0];
#pragma unroll
      for (int j = 1; j < 20; ++j) { mx = fmaxf(mx, p[j]); mn = fminf(mn, p[j]); }
      float e1[20], e2[20], s1 = 0.f, s2 = 0.f;
#pragma unroll
      for (int j = 0; j < 20; ++j) {
        e1[j] = __expf(p[j] - mx); s1 += e1[j];
        e2[j] = __expf(mn - p[j]); s2 += e2[j];
      }
      float r1 = 1.0f / s1, r2 = 1.0f / s2;
      u16* dst = PHI + r * PHS + hh * 40;
#pragma unroll
      for (int j = 0; j < 20; ++j) { dst[j] = f2bf(e1[j] * r1); dst[20 + j] = f2bf(e2[j] * r2); }
    }
    __syncthreads();
    {
      const int h = ct * 4 + w;
      f32x4 kv[3][2];
#pragma unroll
      for (int a = 0; a < 3; ++a)
#pragma unroll
        for (int b = 0; b < 2; ++b) kv[a][b] = f32x4{0.f, 0.f, 0.f, 0.f};
#pragma unroll
      for (int ks = 0; ks < 2; ++ks) {
        bf16x8 vb[2];
#pragma unroll
        for (int n2 = 0; n2 < 2; ++n2) {
          const u16* bp = Cs + (ks * 32 + 8 * g + lq) * CSTR + 160 + 20 * w + n2 * 16 + 4 * lp;
          s16x4 b0 = trread(bp), b1 = trread(bp + 4 * CSTR);
          bf16x8 bf = cat4(b0, b1);
          if (n2 == 1) {
            short fill = (li == 4) ? (short)0x3F80 : (short)0;
            if (li >= 4) {
#pragma unroll
              for (int j = 0; j < 8; ++j) bf[j] = fill;
            }
          }
          vb[n2] = bf;
        }
#pragma unroll
        for (int mt = 0; mt < 3; ++mt) {
          const u16* ap = PHI + (ks * 32 + 8 * g + lq) * PHS + 40 * w + mt * 16 + 4 * lp;
          s16x4 a0 = trread(ap), a1 = trread(ap + 4 * PHS);
          bf16x8 af = cat4(a0, a1);
#pragma unroll
          for (int n2 = 0; n2 < 2; ++n2) kv[mt][n2] = mfma16(af, vb[n2], kv[mt][n2]);
        }
      }
      float* dst = kvpart + ((size_t)mt_blk * H_ + h) * 960;
#pragma unroll
      for (int mt = 0; mt < 3; ++mt)
#pragma unroll
        for (int n2 = 0; n2 < 2; ++n2) {
          int d = n2 * 16 + li;
          if (d < 21) {
#pragma unroll
            for (int i = 0; i < 4; ++i) {
              int f = mt * 16 + 4 * g + i;
              if (f < 40) dst[f * 24 + d] = kv[mt][n2][i];
            }
          }
        }
    }
  }
}

__global__ void __launch_bounds__(256)
attn_apply(const u16* __restrict__ q, const u16* __restrict__ wphi, const float* __restrict__ kvpart,
           u16* __restrict__ o) {
  __shared__ __attribute__((aligned(16))) u16 KVZ[64 * 32];
  __shared__ __attribute__((aligned(16))) u16 PQ[256 * 24];
  __shared__ __attribute__((aligned(16))) u16 PHI[256 * 72];
  const int tid = threadIdx.x, lane = tid & 63, w = tid >> 6;
  const int h = blockIdx.x & 31, rb = blockIdx.x >> 5;
  const int g = lane >> 4, li = lane & 15;
  const int row0 = rb * 256;
  for (int i = tid; i < 64 * 32; i += 256) KVZ[i] = 0;
  __syncthreads();
  for (int idx = tid; idx < 960; idx += 256) {
    int f = idx / 24, d = idx % 24;
    if (d < 21) {
      float s = 0.f;
      for (int rt = 0; rt < 32; ++rt) s += kvpart[((size_t)rt * H_ + h) * 960 + idx];
      KVZ[f * 32 + d] = f2bf(s);
    }
  }
  {
    bf16x8 wb[2];
#pragma unroll
    for (int n2 = 0; n2 < 2; ++n2) {
      int n = n2 * 16 + li;
#pragma unroll
      for (int j = 0; j < 8; ++j) {
        int k = 8 * g + j;
        u16 v = 0;
        if (k < DH_ && n < DH_) v = wphi[(size_t)h * DH_ * DH_ + k * DH_ + n];
        wb[n2][j] = (short)v;
      }
    }
#pragma unroll
    for (int mt = 0; mt < 4; ++mt) {
      int r = w * 64 + mt * 16 + li;
      const u16* ap = q + (size_t)(row0 + r) * D_ + h * DH_ + 8 * g;
      u32x4 av = {0u, 0u, 0u, 0u};
      if (g < 2) { u32x2 a0 = *(const u32x2*)ap, a1 = *(const u32x2*)(ap + 4); av = u32x4{a0[0], a0[1], a1[0], a1[1]}; }
      else if (g == 2) { u32x2 a0 = *(const u32x2*)ap; av[0] = a0[0]; av[1] = a0[1]; }
      bf16x8 af = __builtin_bit_cast(bf16x8, av);
#pragma unroll
      for (int n2 = 0; n2 < 2; ++n2) {
        f32x4 p = mfma16(af, wb[n2], f32x4{0.f, 0.f, 0.f, 0.f});
        int n = n2 * 16 + li;
        if (n < DH_) {
#pragma unroll
          for (int i = 0; i < 4; ++i) PQ[(w * 64 + mt * 16 + 4 * g + i) * 24 + n] = f2bf(p[i]);
        }
      }
    }
  }
  __syncthreads();
  {
    const int r = tid;
    const u16* pp = PQ + r * 24;
    float p[20];
#pragma unroll
    for (int j = 0; j < 20; ++j) p[j] = bf2f(pp[j]);
    float mx = p[0], mn = p[0];
#pragma unroll
    for (int j = 1; j < 20; ++j) { mx = fmaxf(mx, p[j]); mn = fminf(mn, p[j]); }
    float e1[20], e2[20], s1 = 0.f, s2 = 0.f;
#pragma unroll
    for (int j = 0; j < 20; ++j) {
      e1[j] = __expf(p[j] - mx); s1 += e1[j];
      e2[j] = __expf(mn - p[j]); s2 += e2[j];
    }
    float r1 = 1.0f / s1, r2 = 1.0f / s2;
    u16* dst = PHI + r * 72;
#pragma unroll
    for (int j = 0; j < 20; ++j) { dst[j] = f2bf(e1[j] * r1); dst[20 + j] = f2bf(e2[j] * r2); }
#pragma unroll
    for (int j = 40; j < 64; ++j) dst[j] = 0;
  }
  __syncthreads();
  {
    bf16x8 kb[2][2];
#pragma unroll
    for (int ks = 0; ks < 2; ++ks)
#pragma unroll
      for (int n2 = 0; n2 < 2; ++n2)
#pragma unroll
        for (int j = 0; j < 8; ++j) kb[ks][n2][j] = (short)KVZ[(ks * 32 + 8 * g + j) * 32 + n2 * 16 + li];
#pragma unroll
    for (int mt = 0; mt < 4; ++mt) {
      int r = w * 64 + mt * 16 + li;
      f32x4 num0 = {0.f, 0.f, 0.f, 0.f}, num1 = {0.f, 0.f, 0.f, 0.f};
#pragma unroll
      for (int ks = 0; ks < 2; ++ks) {
        bf16x8 af = *(const bf16x8*)(PHI + r * 72 + ks * 32 + 8 * g);
        num0 = mfma16(af, kb[ks][0], num0);
        num1 = mfma16(af, kb[ks][1], num1);
      }
#pragma unroll
      for (int i = 0; i < 4; ++i) {
        float den = __shfl(num1[i], g * 16 + 4);
        den = rbf(rbf(den) + 1e-6f);
        int rr = row0 + w * 64 + mt * 16 + 4 * g + i;
        o[(size_t)rr * D_ + h * DH_ + li] = f2bf(rbf(num0[i]) / den);
        if (li < 4) o[(size_t)rr * D_ + h * DH_ + 16 + li] = f2bf(rbf(num1[i]) / den);
      }
    }
  }
}

__global__ void __launch_bounds__(256)
ln_z2_t(const u16* __restrict__ z2raw, u16* __restrict__ z2nT) {
  __shared__ float stat[64][2];
  __shared__ __attribute__((aligned(16))) u16 T[96 * 72];
  const int tid = threadIdx.x;
  const int rb = blockIdx.x >> 3, cs = blockIdx.x & 7;
  const int row0 = rb * 64;
  {
    const int r = tid >> 2, part = tid & 3;
    const u16* zr = z2raw + (size_t)(row0 + r) * FH_;
    u32x4 xv[24];
#pragma unroll
    for (int i = 0; i < 24; ++i) xv[i] = *(const u32x4*)(zr + (part + 4 * i) * 8);
    float s = 0.f;
#pragma unroll
    for (int i = 0; i < 24; ++i)
#pragma unroll
      for (int j = 0; j < 4; ++j) { s += bf2f(xv[i][j] & 0xffff); s += bf2f(xv[i][j] >> 16); }
    s += __shfl_xor(s, 1); s += __shfl_xor(s, 2);
    const float mean = s * (1.0f / FH_);
    float v = 0.f;
#pragma unroll
    for (int i = 0; i < 24; ++i)
#pragma unroll
      for (int j = 0; j < 4; ++j) {
        float a = bf2f(xv[i][j] & 0xffff) - mean, b = bf2f(xv[i][j] >> 16) - mean;
        v += a * a; v += b * b;
      }
    v += __shfl_xor(v, 1); v += __shfl_xor(v, 2);
    if (part == 0) {
      stat[r][0] = rbf(mean);
      stat[r][1] = 1.0f / rbf(sqrtf(rbf(rbf(v * (1.0f / FH_)) + 1e-5f)));
    }
  }
  __syncthreads();
  for (int i = tid; i < 64 * 12; i += 256) {
    int r = i / 12, c = (i % 12) * 8;
    u32x4 xv = *(const u32x4*)(z2raw + (size_t)(row0 + r) * FH_ + cs * 96 + c);
    float m = stat[r][0], rs = stat[r][1];
#pragma unroll
    for (int j = 0; j < 4; ++j) {
      T[(c + 2 * j) * 72 + r] = f2bf(ln_apply(bf2f(xv[j] & 0xffff), m, rs));
      T[(c + 2 * j + 1) * 72 + r] = f2bf(ln_apply(bf2f(xv[j] >> 16), m, rs));
    }
  }
  __syncthreads();
  for (int i = tid; i < 96 * 8; i += 256) {
    int f = i >> 3, c = (i & 7) * 8;
    u32x4 v = *(const u32x4*)(T + f * 72 + c);
    *(u32x4*)(z2nT + (size_t)(cs * 96 + f) * S_ + row0 + c) = v;
  }
}

template <int NST>
__global__ void __launch_bounds__(256)
gemm_sp(const u16* __restrict__ wsp, const u16* __restrict__ z2nT, const u16* __restrict__ bsp,
        const u16* __restrict__ z1, u16* __restrict__ hout) {
  constexpr int BN = 96, BK = 64;
  constexpr int ACH = BM * 8, BCH = BN * 8, NI = (ACH + BCH) / 256;
  constexpr int STAGE_B = NI * 256 * 16;
  __shared__ __attribute__((aligned(16))) char smem[NST * STAGE_B];
  const int tid = threadIdx.x, lane = tid & 63, w = tid >> 6;
  const int wm = w & 1, wn = w >> 1;
  const int mb = blockIdx.x >> 3, nb = blockIdx.x & 7;
  const int row0 = mb * BM, col0 = nb * BN;
  const int g = lane >> 4, li = lane & 15;
  const u16* src[NI];
#pragma unroll
  for (int i = 0; i < NI; ++i) {
    int c = i * 256 + tid;
    if (i < ACH / 256) { int r = c >> 3, pc = c & 7; src[i] = wsp + (size_t)(row0 + r) * S_ + ((pc ^ (r & 7)) * 8); }
    else { int b = c - ACH; int r = b >> 3, pc = b & 7; src[i] = z2nT + (size_t)(col0 + r) * S_ + ((pc ^ (r & 7)) * 8); }
  }
  auto issue = [&](int kt, int buf) {
#pragma unroll
    for (int i = 0; i < NI; ++i) dma16(src[i] + kt * BK, smem + buf * STAGE_B + (i * 256 + tid) * 16);
  };
  constexpr int KSTEPS = S_ / BK;
#pragma unroll
  for (int s = 0; s < NST - 1; ++s) issue(s, s);
  f32x4 acc[2][3];
#pragma unroll
  for (int a = 0; a < 2; ++a)
#pragma unroll
    for (int b = 0; b < 3; ++b) acc[a][b] = f32x4{0.f, 0.f, 0.f, 0.f};
  int buf = 0;
  for (int kt = 0; kt < KSTEPS; ++kt) {
    if (kt <= KSTEPS - NST + 1) WAIT_VM((NST - 2) * NI); else WAIT_VM(0);
    RAW_BARRIER();
    if (kt + NST - 1 < KSTEPS) { int nb2 = buf + NST - 1; if (nb2 >= NST) nb2 -= NST; issue(kt + NST - 1, nb2); }
    const char* sa = smem + buf * STAGE_B;
    const char* sbp = sa + ACH * 16;
#pragma unroll
    for (int ks = 0; ks < 2; ++ks) {
      bf16x8 af[2], bf[3];
#pragma unroll
      for (int mt = 0; mt < 2; ++mt) { int r = wm * 32 + mt * 16 + li; af[mt] = *(const bf16x8*)(sa + r * 128 + (((ks * 4 + g) ^ (r & 7)) * 16)); }
#pragma unroll
      for (int nt = 0; nt < 3; ++nt) { int r = wn * 48 + nt * 16 + li; bf[nt] = *(const bf16x8*)(sbp + r * 128 + (((ks * 4 + g) ^ (r & 7)) * 16)); }
#pragma unroll
      for (int mt = 0; mt < 2; ++mt)
#pragma unroll
        for (int nt = 0; nt < 3; ++nt) acc[mt][nt] = mfma16(af[mt], bf[nt], acc[mt][nt]);
    }
    if (++buf == NST) buf = 0;
  }
#pragma unroll
  for (int mt = 0; mt < 2; ++mt)
#pragma unroll
    for (int nt = 0; nt < 3; ++nt)
#pragma unroll
      for (int i = 0; i < 4; ++i) {
        int r = row0 + wm * 32 + mt * 16 + 4 * g + i;
        int c = col0 + wn * 48 + nt * 16 + li;
        float gg = rbf(rbf(acc[mt][nt][i]) + bf2f(bsp[r]));
        size_t idx = (size_t)r * FH_ + c;
        hout[idx] = f2bf(bf2f(z1[idx]) * gg);
      }
}

extern "C" void kernel_launch(void* const* d_in, const int* in_sizes, int n_in,
                              void* d_out, int out_size, void* d_ws, size_t ws_size,
                              hipStream_t stream) {
  const u16* A_x = (const u16*)d_in[0];
  const u16* wqkv = (const u16*)d_in[1];
  const u16* wphi = (const u16*)d_in[2];
  const u16* wo = (const u16*)d_in[3];
  const u16* w_in = (const u16*)d_in[5];
  const u16* w_sp = (const u16*)d_in[6];
  const u16* b_sp = (const u16*)d_in[7];
  const u16* w_out = (const u16*)d_in[8];
  u16* out = (u16*)d_out;

  char* ws = (char*)d_ws;
  size_t off = 0;
  auto alloc = [&](size_t bytes) { char* p = ws + off; off += (bytes + 255) & ~(size_t)255; return p; };
  u16* xa = (u16*)alloc((size_t)S_ * D_ * 2);
  u16* xb = (u16*)alloc((size_t)S_ * D_ * 2);
  u16* qb = (u16*)alloc((size_t)S_ * D_ * 2);
  u16* ob = (u16*)alloc((size_t)S_ * D_ * 2);
  float* kvp = (float*)alloc((size_t)32 * H_ * 960 * 4);
  u16* z1 = (u16*)alloc((size_t)S_ * FH_ * 2);
  u16* z2r = (u16*)alloc((size_t)S_ * FH_ * 2);
  u16* z2t = (u16*)alloc((size_t)S_ * FH_ * 2);
  u16* hb = (u16*)alloc((size_t)S_ * FH_ * 2);

  const u16* xin = A_x;
  for (int rep = 0; rep < 2; ++rep) {
    u16* x1 = xa;
    u16* x2 = (rep == 0) ? xb : out;
    gemm_nn<0, 240, 80, 640, 32, 4><<<256, 256, 0, stream>>>(xin, wqkv, 3 * D_, nullptr, qb, nullptr, wphi, kvp);
    attn_apply<<<256, 256, 0, stream>>>(qb, wphi, kvp, ob);
    gemm_nn<1, 80, 80, 640, 64, 4><<<256, 256, 0, stream>>>(ob, wo, D_, xin, x1, nullptr, nullptr, nullptr);
    gemm_nn<2, 192, 48, 640, 32, 4><<<256, 256, 0, stream>>>(x1, w_in, 2 * FH_, nullptr, z1, z2r, nullptr, nullptr);
    ln_z2_t<<<256, 256, 0, stream>>>(z2r, z2t);
    gemm_sp<4><<<256, 256, 0, stream>>>(w_sp, z2t, b_sp, z1, hb);
    gemm_nn<1, 80, 80, 768, 64, 4><<<256, 256, 0, stream>>>(hb, w_out, D_, x1, x2, nullptr, nullptr, nullptr);
    xin = x2;
  }
}
```

```cpp
#include <hip/hip_runtime.h>
#include <hip/hip_bf16.h>
#include <stdint.h>

typedef unsigned short u16;
typedef short bf16x8 __attribute__((ext_vector_type(8)));
typedef short s16x4 __attribute__((ext_vector_type(4)));
typedef float f32x4 __attribute__((ext_vector_type(4)));
typedef unsigned int u32x4 __attribute__((ext_vector_type(4)));
typedef unsigned int u32x2 __attribute__((ext_vector_type(2)));
#define LDS3(T, p) ((__attribute__((address_space(3))) T*)(p))

constexpr int S_ = 2048, D_ = 640, H_ = 32, DH_ = 20, FH_ = 768;

__device__ __forceinline__ float bf2f(u16 v) { return __uint_as_float(((unsigned)v) << 16); }
__device__ __forceinline__ u16 f2bf(float f) { return __builtin_bit_cast(u16, (__bf16)f); }
__device__ __forceinline__ float rbf(float f) { return bf2f(f2bf(f)); }
__device__ __forceinline__ f32x4 mfma16(bf16x8 a, bf16x8 b, f32x4 c) {
  return __builtin_amdgcn_mfma_f32_16x16x32_bf16(a, b, c, 0, 0, 0);
}
__device__ __forceinline__ s16x4 trread(const u16* lds_ptr) {
  return __builtin_amdgcn_ds_read_tr16_b64_v4i16(LDS3(s16x4, lds_ptr));
}
__device__ __forceinline__ bf16x8 cat4(s16x4 a, s16x4 b) {
  bf16x8 r; r[0]=a[0]; r[1]=a[1]; r[2]=a[2]; r[3]=a[3]; r[4]=b[0]; r[5]=b[1]; r[6]=b[2]; r[7]=b[3]; return r;
}

__device__ __forceinline__ float ln_apply(float x, float m_bf, float rs) { return rbf(rbf(x - m_bf) * rs); }

constexpr int BM = 64;
#define WAIT_VM(N) asm volatile("s_waitcnt vmcnt(%0)" :: "n"(N) : "memory")
#define WAIT_LGKM0() asm volatile("s_waitcnt lgkmcnt(0)" ::: "memory")
#define RAW_BARRIER() do { WAIT_LGKM0(); __builtin_amdgcn_s_barrier(); } while (0)
__device__ __forceinline__ void dma16(const u16* src, char* lds_dst) {
  __builtin_amdgcn_global_load_lds((const unsigned*)src, (unsigned*)lds_dst, 16, 0, 0);
}
__device__ __forceinline__ u32x4 ln8(u32x4 x, float m_bf, float rs) {
  u32x4 o;
#pragma unroll
  for (int j = 0; j < 4; ++j) {
    u16 lo = f2bf(ln_apply(bf2f(x[j] & 0xffff), m_bf, rs));
    u16 hi = f2bf(ln_apply(bf2f(x[j] >> 16), m_bf, rs));
    o[j] = (unsigned)lo | ((unsigned)hi << 16);
  }
  return o;
}
__device__ __forceinline__ float gelu_f(float x) {
  float u = 0.7978845608028654f * (x + 0.044715f * x * x * x);
  float t = 1.0f - 2.0f / (__expf(2.0f * u) + 1.0f);
  return 0.5f * x * (1.0f + t);
}

template <int EPI, int BN, int SW, int KDIM, int NST>
__global__ void __launch_bounds__(512)
gemm_nn(const u16* __restrict__ A, const u16* __restrict__ B, int ldb,
        const u16* __restrict__ res, u16* __restrict__ out0, u16* __restrict__ out1,
        const u16* __restrict__ wphi, float* __restrict__ kvpart) {
  constexpr bool ALN = (EPI != 1);
  constexpr int BK = 64;
  constexpr int NT = BN / 16, NTW = (NT + 1) / 2, NTS = SW / 16;
  constexpr int KSTEPS = KDIM / BK;
  constexpr int ACH = BM * BK / 8;
  constexpr int BCH = BK * BN / 8;
  constexpr int NB = (BCH + 255) / 256;
  constexpr int NA = ALN ? 2 : 2;
  constexpr int NVM = NA + NB;
  constexpr int STAGE_B = (ACH + NB * 256) * 16;
  constexpr int CSTR = BN + 8;
  constexpr int EPI0_B = (BM * CSTR + BM * 4 * 24 + BM * 168) * 2;
  constexpr int MAIN_B = NST * STAGE_B + 512;
  constexpr int SMEM_B = (EPI == 0 && EPI0_B > MAIN_B) ? EPI0_B : MAIN_B;
  __shared__ __attribute__((aligned(16))) char smem[SMEM_B];
  float* stat = (float*)(smem + NST * STAGE_B);

  const int tid = threadIdx.x, lane = tid & 63, w = tid >> 6;
  const bool producer = (w >= 4);
  const int wm = w & 1, wn = (w >> 1) & 1;
  constexpr int NCT = 8;
  const int mt_blk = blockIdx.x / NCT, ct = blockIdx.x % NCT;
  const int row0 = mt_blk * BM;
  const int g = lane >> 4, li = lane & 15, lq = li >> 2, lp = li & 3;
  const int pt = tid - 256;

  f32x4 acc[2][NTW];
#pragma unroll
  for (int a = 0; a < 2; ++a)
#pragma unroll
    for (int b = 0; b < NTW; ++b) acc[a][b] = f32x4{0.f, 0.f, 0.f, 0.f};

  const u16* bsrc[NB];
  int binc[NB];
  const u16* asrc[2];
  if (producer) {
#pragma unroll
    for (int i = 0; i < NB; ++i) {
      int b = i * 256 + pt;
      if (b < BCH) {
        int sub = b / (BK * SW / 8), rem = b % (BK * SW / 8);
        int prow = rem / (SW / 8), cc = rem % (SW / 8);
        int row = prow ^ (((prow >> 3) & 1) << 2);
        int col = (EPI == 0) ? (sub * 640 + ct * 80 + cc * 8) : (ct * BN + sub * SW + cc * 8);
        bsrc[i] = B + (size_t)row * ldb + col;
        binc[i] = BK * ldb;
      } else { bsrc[i] = B; binc[i] = 0; }
    }
#pragma unroll
    for (int i = 0; i < 2; ++i) {
      int c = i * 256 + pt; int r = c >> 3, pc = c & 7;
      asrc[i] = A + (size_t)(row0 + r) * KDIM + ((pc ^ (r & 7)) * 8);
    }
  }
  auto issueB = [&](int kt, int buf) {
#pragma unroll
    for (int i = 0; i < NB; ++i) dma16(bsrc[i] + (size_t)kt * binc[i], smem + buf * STAGE_B + (ACH + i * 256 + pt) * 16);
  };
  auto issueA = [&](int kt, int buf) {
#pragma unroll
    for (int i = 0; i < 2; ++i) dma16(asrc[i] + kt * BK, smem + buf * STAGE_B + (i * 256 + pt) * 16);
  };

  u32x4 rres[3];
  if constexpr (EPI == 1) {
    if (producer) {
#pragma unroll
      for (int i = 0; i < 3; ++i) { int c = i * 256 + pt; if (c < 640) { int r = c / 10, cc = c % 10; rres[i] = *(const u32x4*)(res + (size_t)(row0 + r) * D_ + ct * BN + cc * 8); } }
    }
  }

  if constexpr (ALN) {
    if (producer) {
#pragma unroll
      for (int s = 0; s < NST - 1; ++s) issueB(s, s);
    }
    {
      const int r = tid >> 3, part = tid & 7;
      u32x4 xv[10];
      const u16* xr = A + (size_t)(row0 + r) * D_;
#pragma unroll
      for (int i = 0; i < 10; ++i) xv[i] = *(const u32x4*)(xr + (part + 8 * i) * 8);
      float s = 0.f;
#pragma unroll
      for (int i = 0; i < 10; ++i)
#pragma unroll
        for (int j = 0; j < 4; ++j) { s += bf2f(xv[i][j] & 0xffff); s += bf2f(xv[i][j] >> 16); }
      s += __shfl_xor(s, 1); s += __shfl_xor(s, 2); s += __shfl_xor(s, 4);
      const float mean = s * (1.0f / D_);
      float v = 0.f;
#pragma unroll
      for (int i = 0; i < 10; ++i)
#pragma unroll
        for (int j = 0; j < 4; ++j) {
          float a = bf2f(xv[i][j] & 0xffff) - mean, b = bf2f(xv[i][j] >> 16) - mean;
          v += a * a; v += b * b;
        }
      v += __shfl_xor(v, 1); v += __shfl_xor(v, 2); v += __shfl_xor(v, 4);
      if (part == 0) {
        stat[r * 2] = rbf(mean);
        stat[r * 2 + 1] = 1.0f / rbf(sqrtf(rbf(rbf(v * (1.0f / D_)) + 1e-5f)));
      }
    }
    RAW_BARRIER();
  }

  if (producer) {
    if constexpr (ALN) {
      const int ra = pt >> 3;
      const float m0 = stat[ra * 2], rs0 = stat[ra * 2 + 1], m1 = stat[(ra + 32) * 2], rs1 = stat[(ra + 32) * 2 + 1];
      u32x4 a0, a1;
#pragma unroll
      for (int s = 0; s < NST - 1; ++s) {
        a0 = *(const u32x4*)(asrc[0] + s * BK); a1 = *(const u32x4*)(asrc[1] + s * BK);
        *(u32x4*)(smem + s * STAGE_B + pt * 16) = ln8(a0, m0, rs0);
        *(u32x4*)(smem + s * STAGE_B + (256 + pt) * 16) = ln8(a1, m1, rs1);
      }
      a0 = *(const u32x4*)(asrc[0] + (NST - 1) * BK); a1 = *(const u32x4*)(asrc[1] + (NST - 1) * BK);
      int buf = NST - 1;
      for (int kt = 0; kt < KSTEPS; ++kt) {
        if (kt >= NST - 1) { if (kt <= KSTEPS - NST + 1) WAIT_VM((NST - 2) * NVM); else WAIT_VM(0); }
        RAW_BARRIER();
        const int n = kt + NST - 1;
        if (n < KSTEPS) {
          *(u32x4*)(smem + buf * STAGE_B + pt * 16) = ln8(a0, m0, rs0);
          *(u32x4*)(smem + buf * STAGE_B + (256 + pt) * 16) = ln8(a1, m1, rs1);
          if (n + 1 < KSTEPS) { a0 = *(const u32x4*)(asrc[0] + (n + 1) * BK); a1 = *(const u32x4*)(asrc[1] + (n + 1) * BK); }
          issueB(n, buf);
        }
        if (++buf == NST) buf = 0;
      }
    } else {
#pragma unroll
      for (int s = 0; s < NST - 1; ++s) { issueA(s, s); issueB(s, s); }
      int buf = NST - 1;
      for (int kt = 0; kt < KSTEPS; ++kt) {
        if (kt <= KSTEPS - NST + 1) WAIT_VM((NST - 2) * NVM); else WAIT_VM(0);
        RAW_BARRIER();
        if (kt + NST - 1 < KSTEPS) { issueA(kt + NST - 1, buf); issueB(kt + NST - 1, buf); }
        if (++buf == NST) buf = 0;
      }
    }
  } else {
    const int nt0 = wn * NTW;
    const int ntw = (NT - nt0) < NTW ? (NT - nt0) : NTW;
    const int bt0 = (8 * g + 4 * (g & 1) + lq) * (SW * 2) + 8 * lp;
    const int bt1 = (8 * g + 4 * (1 ^ (g & 1)) + lq) * (SW * 2) + 8 * lp;
    int buf = 0;
    for (int kt = 0; kt < KSTEPS; ++kt) {
      RAW_BARRIER();
      const char* sb = smem + buf * STAGE_B;
#pragma unroll
      for (int ks = 0; ks < 2; ++ks) {
        bf16x8 af[2];
#pragma unroll
        for (int mt = 0; mt < 2; ++mt) {
          int r = wm * 32 + mt * 16 + li;
          int pc = (ks * 4 + g) ^ (r & 7);
          af[mt] = *(const bf16x8*)(sb + r * 128 + pc * 16);
        }
        const char* bb = sb + ACH * 16 + ks * 32 * (SW * 2);
#pragma unroll
        for (int nt = 0; nt < NTW; ++nt) {
          if (nt < ntw) {
            int ntg = nt0 + nt;
            int sub = ntg / NTS, n0 = (ntg % NTS) * 16;
            const char* bp = bb + sub * (BK * SW * 2) + n0 * 2;
            s16x4 b0 = trread((const u16*)(bp + bt0));
            s16x4 b1 = trread((const u16*)(bp + bt1));
            bf16x8 bf = cat4(b0, b1);
            acc[0][nt] = mfma16(af[0], bf, acc[0][nt]);
            acc[1][nt] = mfma16(af[1], bf, acc[1][nt]);
          }
        }
      }
      if (++buf == NST) buf = 0;
    }
  }
  __syncthreads();

  u16* Cs = (u16*)smem;
  if (!producer) {
    const int nt0 = wn * NTW;
    const int ntw = (NT - nt0) < NTW ? (NT - nt0) : NTW;
#pragma unroll
    for (int mt = 0; mt < 2; ++mt)
#pragma unroll
      for (int nt = 0; nt < NTW; ++nt)
        if (nt < ntw) {
#pragma unroll
          for (int i = 0; i < 4; ++i) {
            int r = wm * 32 + mt * 16 + 4 * g + i;
            int c = (nt0 + nt) * 16 + li;
            Cs[r * CSTR + c] = f2bf(acc[mt][nt][i]);
          }
        }
  }
  __syncthreads();
  if constexpr (EPI == 1) {
    if (producer) {
#pragma unroll
      for (int i = 0; i < 3; ++i) {
        int c = i * 256 + pt;
        if (c < 640) {
          int r = c / 10, cc = c % 10;
          u32x4 v = *(const u32x4*)(Cs + r * CSTR + cc * 8), o;
#pragma unroll
          for (int j = 0; j < 4; ++j) {
            u16 lo = f2bf(bf2f(rres[i][j] & 0xffff) + bf2f(v[j] & 0xffff));
            u16 hi = f2bf(bf2f(rres[i][j] >> 16) + bf2f(v[j] >> 16));
            o[j] = (unsigned)lo | ((unsigned)hi << 16);
          }
          *(u32x4*)(out0 + (size_t)(row0 + r) * D_ + ct * BN + cc * 8) = o;
        }
      }
    }
  } else if constexpr (EPI == 2) {
#pragma unroll
    for (int i = 0; i < 3; ++i) {
      int c = i * 512 + tid;
      int r = c / 24, cc = c % 24;
      u32x4 v = *(const u32x4*)(Cs + r * CSTR + cc * 8), o;
#pragma unroll
      for (int j = 0; j < 4; ++j) {
        u16 lo = f2bf(gelu_f(bf2f(v[j] & 0xffff)));
        u16 hi = f2bf(gelu_f(bf2f(v[j] >> 16)));
        o[j] = (unsigned)lo | ((unsigned)hi << 16);
      }
      int col = ct * BN + cc * 8;
      if (col < FH_) *(u32x4*)(out0 + (size_t)(row0 + r) * FH_ + col) = o;
      else *(u32x4*)(out1 + (size_t)(row0 + r) * FH_ + (col - FH_)) = o;
    }
  } else {
    u16* PK = Cs + BM * CSTR;
    u16* PHI = PK + BM * 4 * 24;
    constexpr int PHS = 168;
    for (int i = tid; i < BM * 10; i += 512) {
      int r = i / 10, c = (i % 10) * 8;
      u32x4 v = *(const u32x4*)(Cs + r * CSTR + c);
      *(u32x4*)(out0 + (size_t)(row0 + r) * D_ + ct * 80 + c) = v;
    }
    {
      const int hw = w & 3, mh = w >> 2;
      const int h = ct * 4 + hw;
      bf16x8 wb[2];
#pragma unroll
      for (int n2 = 0; n2 < 2; ++n2) {
        int n = n2 * 16 + li;
#pragma unroll
        for (int j = 0; j < 8; ++j) {
          int k = 8 * g + j;
          u16 v = 0;
          if (k < DH_ && n < DH_) v = wphi[(size_t)h * DH_ * DH_ + k * DH_ + n];
          wb[n2][j] = (short)v;
        }
      }
#pragma unroll
      for (int mt2 = 0; mt2 < 2; ++mt2) {
        int mt = mh * 2 + mt2;
        const u16* ap = Cs + (mt * 16 + li) * CSTR + 80 + 20 * hw + 8 * g;
        u32x2 a0 = *(const u32x2*)ap, a1 = *(const u32x2*)(ap + 4);
        u32x4 av = {a0[0], a0[1], a1[0], a1[1]};
        bf16x8 af = __builtin_bit_cast(bf16x8, av);
#pragma unroll
        for (int n2 = 0; n2 < 2; ++n2) {
          f32x4 p = mfma16(af, wb[n2], f32x4{0.f, 0.f, 0.f, 0.f});
          int n = n2 * 16 + li;
          if (n < DH_) {
#pragma unroll
            for (int i = 0; i < 4; ++i) PK[((mt * 16 + 4 * g + i) * 4 + hw) * 24 + n] = f2bf(p[i]);
          }
        }
      }
    }
    __syncthreads();
    if (tid < 256) {
      const int r = tid >> 2, hh = tid & 3;
      const u16* pp = PK + (r * 4 + hh) * 24;
      float p[20];
#pragma unroll
      for (int j = 0; j < 20; ++j) p[j] = bf2f(pp[j]);
      float mx = p[0], mn = p[0];
#pragma unroll
      for (int j = 1; j < 20; ++j) { mx = fmaxf(mx, p[j]); mn = fminf(mn, p[j]); }
      float e1[20], e2[20], s1 = 0.f, s2 = 0.f;
#pragma unroll
      for (int j = 0; j < 20; ++j) {
        e1[j] = __expf(p[j] - mx); s1 += e1[j];
        e2[j] = __expf(mn - p[j]); s2 += e2[j];
      }
      float r1 = 1.0f / s1, r2 = 1.0f / s2;
      u16* dst = PHI + r * PHS + hh * 40;
#pragma unroll
      for (int j = 0; j < 20; ++j) { dst[j] = f2bf(e1[j] * r1); dst[20 + j] = f2bf(e2[j] * r2); }
    }
    __syncthreads();
    if (w < 4) {
      const int h = ct * 4 + w;
      f32x4 kv[3][2];
#pragma unroll
      for (int a = 0; a < 3; ++a)
#pragma unroll
        for (int b = 0; b < 2; ++b) kv[a][b] = f32x4{0.f, 0.f, 0.f, 0.f};
#pragma unroll
      for (int ks = 0; ks < 2; ++ks) {
        bf16x8 vb[2];
#pragma unroll
        for (int n2 = 0; n2 < 2; ++n2) {
          const u16* bp = Cs + (ks * 32 + 8 * g + lq) * CSTR + 160 + 20 * w + n2 * 16 + 4 * lp;
          s16x4 b0 = trread(bp), b1 = trread(bp + 4 * CSTR);
          bf16x8 bf = cat4(b0, b1);
          if (n2 == 1) {
            short fill = (li == 4) ? (short)0x3F80 : (short)0;
            if (li >= 4) {
#pragma unroll
              for (int j = 0; j < 8; ++j) bf[j] = fill;
            }
          }
          vb[n2] = bf;
        }
#pragma unroll
        for (int mt = 0; mt < 3; ++mt) {
          const u16* ap = PHI + (ks * 32 + 8 * g + lq) * PHS + 40 * w + mt * 16 + 4 * lp;
          s16x4 a0 = trread(ap), a1 = trread(ap + 4 * PHS);
          bf16x8 af = cat4(a0, a1);
#pragma unroll
          for (int n2 = 0; n2 < 2; ++n2) kv[mt][n2] = mfma16(af, vb[n2], kv[mt][n2]);
        }
      }
      float* dst = kvpart + ((size_t)mt_blk * H_ + h) * 960;
#pragma unroll
      for (int mt = 0; mt < 3; ++mt)
#pragma unroll
        for (int n2 = 0; n2 < 2; ++n2) {
          int d = n2 * 16 + li;
          if (d < 21) {
#pragma unroll
            for (int i = 0; i < 4; ++i) {
              int f = mt * 16 + 4 * g + i;
              if (f < 40) dst[f * 24 + d] = kv[mt][n2][i];
            }
          }
        }
    }
  }
}

__global__ void __launch_bounds__(256)
attn_apply(const u16* __restrict__ q, const u16* __restrict__ wphi, const float* __restrict__ kvpart,
           u16* __restrict__ o) {
  __shared__ __attribute__((aligned(16))) u16 KVZ[64 * 32];
  __shared__ __attribute__((aligned(16))) u16 PQ[256 * 24];
  __shared__ __attribute__((aligned(16))) u16 PHI[256 * 72];
  const int tid = threadIdx.x, lane = tid & 63, w = tid >> 6;
  const int h = blockIdx.x & 31, rb = blockIdx.x >> 5;
  const int g = lane >> 4, li = lane & 15;
  const int row0 = rb * 256;
  for (int i = tid; i < 64 * 32 / 2; i += 256) ((unsigned*)KVZ)[i] = 0;
  u32x4 qa[4];
#pragma unroll
  for (int mt = 0; mt < 4; ++mt) {
    int r = w * 64 + mt * 16 + li;
    const u16* ap = q + (size_t)(row0 + r) * D_ + h * DH_ + 8 * g;
    u32x4 av = {0u, 0u, 0u, 0u};
    if (g < 2) { u32x2 a0 = *(const u32x2*)ap, a1 = *(const u32x2*)(ap + 4); av = u32x4{a0[0], a0[1], a1[0], a1[1]}; }
    else if (g == 2) { u32x2 a0 = *(const u32x2*)ap; av[0] = a0[0]; av[1] = a0[1]; }
    qa[mt] = av;
  }
  bf16x8 wb[2];
#pragma unroll
  for (int n2 = 0; n2 < 2; ++n2) {
    int n = n2 * 16 + li;
#pragma unroll
    for (int j = 0; j < 8; ++j) {
      int k = 8 * g + j;
      u16 v = 0;
      if (k < DH_ && n < DH_) v = wphi[(size_t)h * DH_ * DH_ + k * DH_ + n];
      wb[n2][j] = (short)v;
    }
  }
  __syncthreads();
  if (tid < 240) {
    const f32x4* src = (const f32x4*)(kvpart + (size_t)h * 960) + tid;
    f32x4 s = {0.f, 0.f, 0.f, 0.f};
#pragma unroll
    for (int b = 0; b < 4; ++b) {
      f32x4 v[8];
#pragma unroll
      for (int r = 0; r < 8; ++r) v[r] = src[(size_t)(b * 8 + r) * (H_ * 960 / 4)];
#pragma unroll
      for (int r = 0; r < 8; ++r) s += v[r];
    }
    int f = tid / 6, d0 = (tid % 6) * 4;
#pragma unroll
    for (int j = 0; j < 4; ++j) if (d0 + j < 21) KVZ[f * 32 + d0 + j] = f2bf(s[j]);
  }
#pragma unroll
  for (int mt = 0; mt < 4; ++mt) {
    bf16x8 af = __builtin_bit_cast(bf16x8, qa[mt]);
#pragma unroll
    for (int n2 = 0; n2 < 2; ++n2) {
      f32x4 p = mfma16(af, wb[n2], f32x4{0.f, 0.f, 0.f, 0.f});
      int n = n2 * 16 + li;
      if (n < DH_) {
#pragma unroll
        for (int i = 0; i < 4; ++i) PQ[(w * 64 + mt * 16 + 4 * g + i) * 24 + n] = f2bf(p[i]);
      }
    }
  }
  __syncthreads();
  {
    const int r = tid;
    const u16* pp = PQ + r * 24;
    float p[20];
#pragma unroll
    for (int j = 0; j < 20; ++j) p[j] = bf2f(pp[j]);
    float mx = p[0], mn = p[0];
#pragma unroll
    for (int j = 1; j < 20; ++j) { mx = fmaxf(mx, p[j]); mn = fminf(mn, p[j]); }
    float e1[20], e2[20], s1 = 0.f, s2 = 0.f;
#pragma unroll
    for (int j = 0; j < 20; ++j) {
      e1[j] = __expf(p[j] - mx); s1 += e1[j];
      e2[j] = __expf(mn - p[j]); s2 += e2[j];
    }
    float r1 = 1.0f / s1, r2 = 1.0f / s2;
    u16* dst = PHI + r * 72;
#pragma unroll
    for (int j = 0; j < 20; ++j) { dst[j] = f2bf(e1[j] * r1); dst[20 + j] = f2bf(e2[j] * r2); }
#pragma unroll
    for (int j = 40; j < 64; j += 2) *(unsigned*)(dst + j) = 0u;
  }
  __syncthreads();
  {
    bf16x8 kb[2][2];
#pragma unroll
    for (int ks = 0; ks < 2; ++ks)
#pragma unroll
      for (int n2 = 0; n2 < 2; ++n2)
#pragma unroll
        for (int j = 0; j < 8; ++j) kb[ks][n2][j] = (short)KVZ[(ks * 32 + 8 * g + j) * 32 + n2 * 16 + li];
#pragma unroll
    for (int mt = 0; mt < 4; ++mt) {
      int r = w * 64 + mt * 16 + li;
      f32x4 num0 = {0.f, 0.f, 0.f, 0.f}, num1 = {0.f, 0.f, 0.f, 0.f};
#pragma unroll
      for (int ks = 0; ks < 2; ++ks) {
        bf16x8 af = *(const bf16x8*)(PHI + r * 72 + ks * 32 + 8 * g);
        num0 = mfma16(af, kb[ks][0], num0);
        num1 = mfma16(af, kb[ks][1], num1);
      }
#pragma unroll
      for (int i = 0; i < 4; ++i) {
        float den = __shfl(num1[i], g * 16 + 4);
        den = rbf(rbf(den) + 1e-6f);
        int rr = w * 64 + mt * 16 + 4 * g + i;
        PQ[rr * 24 + li] = f2bf(rbf(num0[i]) / den);
        if (li < 4) PQ[rr * 24 + 16 + li] = f2bf(rbf(num1[i]) / den);
      }
    }
  }
  __syncthreads();
  for (int i = tid; i < 256 * 5; i += 256) {
    int r = i / 5, p = i % 5;
    u32x2 v = *(const u32x2*)(PQ + r * 24 + p * 4);
    *(u32x2*)(o + (size_t)(row0 + r) * D_ + h * DH_ + p * 4) = v;
  }
}

__global__ void __launch_bounds__(256)
ln_z2_t(const u16* __restrict__ z2raw, u16* __restrict__ z2nT) {
  __shared__ float stat[64][2];
  __shared__ __attribute__((aligned(16))) u16 T[96 * 72];
  const int tid = threadIdx.x;
  const int rb = blockIdx.x >> 3, cs = blockIdx.x & 7;
  const int row0 = rb * 64;
  {
    const int r = tid >> 2, part = tid & 3;
    const u16* zr = z2raw + (size_t)(row0 + r) * FH_;
    u32x4 xv[24];
#pragma unroll
    for (int i = 0; i < 24; ++i) xv[i] = *(const u32x4*)(zr + (part + 4 * i) * 8);
    float s = 0.f;
#pragma unroll
    for (int i = 0; i < 24; ++i)
#pragma unroll
      for (int j = 0; j < 4; ++j) { s += bf2f(xv[i][j] & 0xffff); s += bf2f(xv[i][j] >> 16); }
    s += __shfl_xor(s, 1); s += __shfl_xor(s, 2);
    const float mean = s * (1.0f / FH_);
    float v = 0.f;
#pragma unroll
    for (int i = 0; i < 24; ++i)
#pragma unroll
      for (int j = 0; j < 4; ++j) {
        float a = bf2f(xv[i][j] & 0xffff) - mean, b = bf2f(xv[i][j] >> 16) - mean;
        v += a * a; v += b * b;
      }
    v += __shfl_xor(v, 1); v += __shfl_xor(v, 2);
    if (part == 0) {
      stat[r][0] = rbf(mean);
      stat[r][1] = 1.0f / rbf(sqrtf(rbf(rbf(v * (1.0f / FH_)) + 1e-5f)));
    }
  }
  __syncthreads();
  for (int i = tid; i < 64 * 12; i += 256) {
    int r = i / 12, c = (i % 12) * 8;
    u32x4 xv = *(const u32x4*)(z2raw + (size_t)(row0 + r) * FH_ + cs * 96 + c);
    float m = stat[r][0], rs = stat[r][1];
#pragma unroll
    for (int j = 0; j < 4; ++j) {
      T[(c + 2 * j) * 72 + r] = f2bf(ln_apply(bf2f(xv[j] & 0xffff), m, rs));
      T[(c + 2 * j + 1) * 72 + r] = f2bf(ln_apply(bf2f(xv[j] >> 16), m, rs));
    }
  }
  __syncthreads();
  for (int i = tid; i < 96 * 8; i += 256) {
    int f = i >> 3, c = (i & 7) * 8;
    u32x4 v = *(const u32x4*)(T + f * 72 + c);
    *(u32x4*)(z2nT + (size_t)(cs * 96 + f) * S_ + row0 + c) = v;
  }
}

template <int BK, int NST>
__global__ void __launch_bounds__(512)
gemm_sp(const u16* __restrict__ wsp, const u16* __restrict__ z2nT, const u16* __restrict__ bsp,
        const u16* __restrict__ z1, u16* __restrict__ hout) {
  constexpr int BN = 96, CPR = BK / 8;
  constexpr int ACH = BM * CPR, BCH = BN * CPR, NI = (ACH + BCH) / 256;
  static_assert((ACH + BCH) % 256 == 0 && ACH % 256 == 0, "");
  constexpr int STAGE_B = NI * 256 * 16;
  constexpr int CSTR = BN + 8;
  __shared__ __attribute__((aligned(16))) char smem[NST * STAGE_B];
  const int tid = threadIdx.x, lane = tid & 63, w = tid >> 6;
  const bool producer = (w >= 4);
  const int wm = w & 1, wn = (w >> 1) & 1;
  const int mb = blockIdx.x >> 3, nb = blockIdx.x & 7;
  const int row0 = mb * BM, col0 = nb * BN;
  const int g = lane >> 4, li = lane & 15;
  const int pt = tid - 256;
  constexpr int KSTEPS = S_ / BK;
  f32x4 acc[2][3];
#pragma unroll
  for (int a = 0; a < 2; ++a)
#pragma unroll
    for (int b = 0; b < 3; ++b) acc[a][b] = f32x4{0.f, 0.f, 0.f, 0.f};
  u32x4 zr[3]; float bb[3];
  if (producer) {
#pragma unroll
    for (int i = 0; i < 3; ++i) { int c = i * 256 + pt; int r = c / 12, cc = c % 12; zr[i] = *(const u32x4*)(z1 + (size_t)(row0 + r) * FH_ + col0 + cc * 8); bb[i] = bf2f(bsp[row0 + r]); }
    const u16* src[NI];
#pragma unroll
    for (int i = 0; i < NI; ++i) {
      int c = i * 256 + pt;
      if (i < ACH / 256) { int r = c / CPR, pc = c % CPR; src[i] = wsp + (size_t)(row0 + r) * S_ + (((pc & ~7) | ((pc ^ r) & 7)) * 8); }
      else { int b = c - ACH; int r = b / CPR, pc = b % CPR; src[i] = z2nT + (size_t)(col0 + r) * S_ + (((pc & ~7) | ((pc ^ r) & 7)) * 8); }
    }
    auto issue = [&](int kt, int buf) {
#pragma unroll
      for (int i = 0; i < NI; ++i) dma16(src[i] + kt * BK, smem + buf * STAGE_B + (i * 256 + pt) * 16);
    };
#pragma unroll
    for (int s = 0; s < NST - 1; ++s) issue(s, s);
    int buf = NST - 1;
    for (int kt = 0; kt < KSTEPS; ++kt) {
      if (kt <= KSTEPS - NST + 1) WAIT_VM((NST - 2) * NI); else WAIT_VM(0);
      RAW_BARRIER();
      if (kt + NST - 1 < KSTEPS) issue(kt + NST - 1, buf);
      if (++buf == NST) buf = 0;
    }
  } else {
    int buf = 0;
    for (int kt = 0; kt < KSTEPS; ++kt) {
      RAW_BARRIER();
      const char* sa = smem + buf * STAGE_B;
      const char* sbp = sa + ACH * 16;
#pragma unroll
      for (int ks = 0; ks < BK / 32; ++ks) {
        bf16x8 af[2], bf[3];
        int c = ks * 4 + g;
#pragma unroll
        for (int mt = 0; mt < 2; ++mt) { int r = wm * 32 + mt * 16 + li; af[mt] = *(const bf16x8*)(sa + r * (BK * 2) + (((c & ~7) | ((c ^ r) & 7)) * 16)); }
#pragma unroll
        for (int nt = 0; nt < 3; ++nt) { int r = wn * 48 + nt * 16 + li; bf[nt] = *(const bf16x8*)(sbp + r * (BK * 2) + (((c & ~7) | ((c ^ r) & 7)) * 16)); }
#pragma unroll
        for (int mt = 0; mt < 2; ++mt)
#pragma unroll
          for (int nt = 0; nt < 3; ++nt) acc[mt][nt] = mfma16(af[mt], bf[nt], acc[mt][nt]);
      }
      if (++buf == NST) buf = 0;
    }
  }
  __syncthreads();
  u16* Cs = (u16*)smem;
  if (!producer) {
#pragma unroll
    for (int mt = 0; mt < 2; ++mt)
#pragma unroll
      for (int nt = 0; nt < 3; ++nt)
#pragma unroll
        for (int i = 0; i < 4; ++i) Cs[(wm * 32 + mt * 16 + 4 * g + i) * CSTR + wn * 48 + nt * 16 + li] = f2bf(acc[mt][nt][i]);
  }
  __syncthreads();
  if (producer) {
#pragma unroll
    for (int i = 0; i < 3; ++i) {
      int c = i * 256 + pt; int r = c / 12, cc = c % 12;
      u32x4 v = *(const u32x4*)(Cs + r * CSTR + cc * 8), o;
#pragma unroll
      for (int j = 0; j < 4; ++j) {
        u16 lo = f2bf(bf2f(zr[i][j] & 0xffff) * rbf(bf2f(v[j] & 0xffff) + bb[i]));
        u16 hi = f2bf(bf2f(zr[i][j] >> 16) * rbf(bf2f(v[j] >> 16) + bb[i]));
        o[j] = (unsigned)lo | ((unsigned)hi << 16);
      }
      *(u32x4*)(hout + (size_t)(row0 + r) * FH_ + col0 + cc * 8) = o;
    }
  }
}

extern "C" void kernel_launch(void* const* d_in, const int* in_sizes, int n_in,
                              void* d_out, int out_size, void* d_ws, size_t ws_size,
                              hipStream_t stream) {
  const u16* A_x = (const u16*)d_in[0];
  const u16* wqkv = (const u16*)d_in[1];
  const u16* wphi = (const u16*)d_in[2];
  const u16* wo = (const u16*)d_in[3];
  const u16* w_in = (const u16*)d_in[5];
  const u16* w_sp = (const u16*)d_in[6];
  const u16* b_sp = (const u16*)d_in[7];
  const u16* w_out = (const u16*)d_in[8];
  u16* out = (u16*)d_out;

  char* ws = (char*)d_ws;
  size_t off = 0;
  auto alloc = [&](size_t bytes) { char* p = ws + off; off += (bytes + 255) & ~(size_t)255; return p; };
  u16* xa = (u16*)alloc((size_t)S_ * D_ * 2);
  u16* xb = (u16*)alloc((size_t)S_ * D_ * 2);
  u16* qb = (u16*)alloc((size_t)S_ * D_ * 2);
  u16* ob = (u16*)alloc((size_t)S_ * D_ * 2);
  float* kvp = (float*)alloc((size_t)32 * H_ * 960 * 4);
  u16* z1 = (u16*)alloc((size_t)S_ * FH_ * 2);
  u16* z2r = (u16*)alloc((size_t)S_ * FH_ * 2);
  u16* z2t = (u16*)alloc((size_t)S_ * FH_ * 2);
  u16* hb = (u16*)alloc((size_t)S_ * FH_ * 2);

  const u16* xin = A_x;
  for (int rep = 0; rep < 2; ++rep) {
    u16* x1 = xa;
    u16* x2 = (rep == 0) ? xb : out;
    gemm_nn<0, 240, 80, 640, 3><<<256, 512, 0, stream>>>(xin, wqkv, 3 * D_, nullptr, qb, nullptr, wphi, kvp);
    attn_apply<<<256, 256, 0, stream>>>(qb, wphi, kvp, ob);
    gemm_nn<1, 80, 80, 640, 4><<<256, 512, 0, stream>>>(ob, wo, D_, xin, x1, nullptr, nullptr, nullptr);
    gemm_nn<2, 192, 48, 640, 4><<<256, 512, 0, stream>>>(x1, w_in, 2 * FH_, nullptr, z1, z2r, nullptr, nullptr);
    ln_z2_t<<<256, 256, 0, stream>>>(z2r, z2t);
    gemm_sp<64, 4><<<256, 512, 0, stream>>>(w_sp, z2t, b_sp, z1, hb);
    gemm_nn<1, 80, 80, 768, 4><<<256, 512, 0, stream>>>(hb, w_out, D_, x1, x2, nullptr, nullptr, nullptr);
    xin = x2;
  }
}
```

```cpp
#include <hip/hip_runtime.h>
#include <hip/hip_bf16.h>
#include <stdint.h>

typedef unsigned short u16;
typedef short bf16x8 __attribute__((ext_vector_type(8)));
typedef short s16x4 __attribute__((ext_vector_type(4)));
typedef float f32x4 __attribute__((ext_vector_type(4)));
typedef unsigned int u32x4 __attribute__((ext_vector_type(4)));
typedef unsigned int u32x2 __attribute__((ext_vector_type(2)));
#define LDS3(T, p) ((__attribute__((address_space(3))) T*)(p))

constexpr int S_ = 2048, D_ = 640, H_ = 32, DH_ = 20, FH_ = 768;

__device__ __forceinline__ float bf2f(u16 v) { return __uint_as_float(((unsigned)v) << 16); }
__device__ __forceinline__ u16 f2bf(float f) { return __builtin_bit_cast(u16, (__bf16)f); }
__device__ __forceinline__ float rbf(float f) { return bf2f(f2bf(f)); }
__device__ __forceinline__ f32x4 mfma16(bf16x8 a, bf16x8 b, f32x4 c) {
  return __builtin_amdgcn_mfma_f32_16x16x32_bf16(a, b, c, 0, 0, 0);
}
__device__ __forceinline__ s16x4 trread(const u16* lds_ptr) {
  return __builtin_amdgcn_ds_read_tr16_b64_v4i16(LDS3(s16x4, lds_ptr));
}
__device__ __forceinline__ bf16x8 cat4(s16x4 a, s16x4 b) {
  bf16x8 r; r[0]=a[0]; r[1]=a[1]; r[2]=a[2]; r[3]=a[3]; r[4]=b[0]; r[5]=b[1]; r[6]=b[2]; r[7]=b[3]; return r;
}

__device__ __forceinline__ float ln_apply(float x, float m_bf, float rs) { return rbf(rbf(x - m_bf) * rs); }

constexpr int BM = 64;
#define WAIT_VM(N) asm volatile("s_waitcnt vmcnt(%0)" :: "n"(N) : "memory")
#define WAIT_LGKM0() asm volatile("s_waitcnt lgkmcnt(0)" ::: "memory")
#define RAW_BARRIER() do { WAIT_LGKM0(); __builtin_amdgcn_s_barrier(); } while (0)
__device__ __forceinline__ void dma16(const u16* src, char* lds_dst) {
  __builtin_amdgcn_global_load_lds((const unsigned*)src, (unsigned*)lds_dst, 16, 0, 0);
}
__device__ __forceinline__ u32x4 ln8(u32x4 x, float m_bf, float rs) {
  u32x4 o;
#pragma unroll
  for (int j = 0; j < 4; ++j) {
    u16 lo = f2bf(ln_apply(bf2f(x[j] & 0xffff), m_bf, rs));
    u16 hi = f2bf(ln_apply(bf2f(x[j] >> 16), m_bf, rs));
    o[j] = (unsigned)lo | ((unsigned)hi << 16);
  }
  return o;
}
__device__ __forceinline__ float gelu_f(float x) {
  float u2 = x * (1.5957691216057308f + 0.07135481282989f * x * x);
  return x * __builtin_amdgcn_rcpf(1.0f + __expf(-u2));
}

template <int EPI, bool ALN, int BN, int SW, int KDIM, int NST>
__global__ void __launch_bounds__(512)
gemm_nn(const u16* __restrict__ A, const u16* __restrict__ B, int ldb,
        const u16* __restrict__ res, u16* __restrict__ out0, u16* __restrict__ out1,
        const u16* __restrict__ wphi, float* __restrict__ kvpart) {
  constexpr int BK = 64;
  constexpr int NT = BN / 16, NTW = (NT + 1) / 2, NTS = SW / 16;
  constexpr int KSTEPS = KDIM / BK;
  constexpr int ACH = BM * BK / 8;
  constexpr int BCH = BK * BN / 8;
  constexpr int NBT = ALN ? 128 : 256;
  constexpr int NB = (BCH + NBT - 1) / NBT;
  constexpr int NVM = ALN ? NB : (NB + 2);
  constexpr int STAGE_B = (ACH + NB * NBT) * 16;
  constexpr int CSTR = BN + 8;
  constexpr int EPI0_B = (BM * CSTR + BM * 4 * 24 + BM * 168) * 2;
  constexpr int MAIN_B = NST * STAGE_B + 512;
  constexpr int SMEM_B = (EPI == 0 && EPI0_B > MAIN_B) ? EPI0_B : MAIN_B;
  __shared__ __attribute__((aligned(16))) char smem[SMEM_B];
  float* stat = (float*)(smem + NST * STAGE_B);

  const int tid = threadIdx.x, lane = tid & 63, w = tid >> 6;
  const bool producer = (w >= 4);
  const int wm = w & 1, wn = (w >> 1) & 1;
  constexpr int NCT = 8;
  const int mt_blk = (blockIdx.x & 7) * 4 + ((blockIdx.x >> 3) & 3), ct = blockIdx.x >> 5;
  const int row0 = mt_blk * BM;
  const int g = lane >> 4, li = lane & 15, lq = li >> 2, lp = li & 3;
  const int pt = tid - 256;

  f32x4 acc[2][NTW];
#pragma unroll
  for (int a = 0; a < 2; ++a)
#pragma unroll
    for (int b = 0; b < NTW; ++b) acc[a][b] = f32x4{0.f, 0.f, 0.f, 0.f};

  const u16* bsrc[NB];
  int binc[NB];
  const u16* asrc[2];
  if (producer) {
#pragma unroll
    for (int i = 0; i < NB; ++i) {
      int b = i * NBT + pt;
      if (b < BCH) {
        int sub = b / (BK * SW / 8), rem = b % (BK * SW / 8);
        int prow = rem / (SW / 8), cc = rem % (SW / 8);
        int row = prow ^ (((prow >> 3) & 1) << 2);
        int col = (EPI == 0) ? (sub * 640 + ct * 80 + cc * 8) : (ct * BN + sub * SW + cc * 8);
        bsrc[i] = B + (size_t)row * ldb + col;
        binc[i] = BK * ldb;
      } else { bsrc[i] = B; binc[i] = 0; }
    }
#pragma unroll
    for (int i = 0; i < 2; ++i) {
      int c = i * 256 + pt; int r = c >> 3, pc = c & 7;
      asrc[i] = A + (size_t)(row0 + r) * KDIM + ((pc ^ (r & 7)) * 8);
    }
  }
  auto issueB = [&](int kt, int buf) {
#pragma unroll
    for (int i = 0; i < NB; ++i) dma16(bsrc[i] + (size_t)kt * binc[i], smem + buf * STAGE_B + (ACH + i * NBT + pt) * 16);
  };
  auto issueA = [&](int kt, int buf) {
#pragma unroll
    for (int i = 0; i < 2; ++i) dma16(asrc[i] + kt * BK, smem + buf * STAGE_B + (i * 256 + pt) * 16);
  };

  u32x4 rres[3];
  if constexpr (EPI == 1) {
    if (!producer) {
#pragma unroll
      for (int i = 0; i < 3; ++i) { int c = i * 256 + tid; if (c < 640) { int r = c / 10, cc = c % 10; rres[i] = *(const u32x4*)(res + (size_t)(row0 + r) * D_ + ct * BN + cc * 8); } }
    }
  }

  if constexpr (ALN) {
    if (w == 4 || w == 5) {
#pragma unroll
      for (int s = 0; s < NST - 1; ++s) issueB(s, s);
    }
    {
      const int r = tid >> 3, part = tid & 7;
      u32x4 xv[10];
      const u16* xr = A + (size_t)(row0 + r) * D_;
#pragma unroll
      for (int i = 0; i < 10; ++i) xv[i] = *(const u32x4*)(xr + (part + 8 * i) * 8);
      float s = 0.f;
#pragma unroll
      for (int i = 0; i < 10; ++i)
#pragma unroll
        for (int j = 0; j < 4; ++j) { s += bf2f(xv[i][j] & 0xffff); s += bf2f(xv[i][j] >> 16); }
      s += __shfl_xor(s, 1); s += __shfl_xor(s, 2); s += __shfl_xor(s, 4);
      const float mean = s * (1.0f / D_);
      float v = 0.f;
#pragma unroll
      for (int i = 0; i < 10; ++i)
#pragma unroll
        for (int j = 0; j < 4; ++j) {
          float a = bf2f(xv[i][j] & 0xffff) - mean, b = bf2f(xv[i][j] >> 16) - mean;
          v += a * a; v += b * b;
        }
      v += __shfl_xor(v, 1); v += __shfl_xor(v, 2); v += __shfl_xor(v, 4);
      if (part == 0) {
        stat[r * 2] = rbf(mean);
        stat[r * 2 + 1] = 1.0f / rbf(sqrtf(rbf(rbf(v * (1.0f / D_)) + 1e-5f)));
      }
    }
    RAW_BARRIER();
  }

  if (producer) {
    if constexpr (ALN) {
      if (w >= 6) {
        const int at = tid - 384, ra = at >> 3, pc = at & 7;
        float mm[4], rr[4];
        const u16* ap[4];
#pragma unroll
        for (int i = 0; i < 4; ++i) {
          int r = ra + 16 * i;
          mm[i] = stat[r * 2]; rr[i] = stat[r * 2 + 1];
          ap[i] = A + (size_t)(row0 + r) * KDIM + ((pc ^ (r & 7)) * 8);
        }
        u32x4 av[4];
#pragma unroll
        for (int s = 0; s < NST - 1; ++s) {
#pragma unroll
          for (int i = 0; i < 4; ++i) av[i] = *(const u32x4*)(ap[i] + s * BK);
#pragma unroll
          for (int i = 0; i < 4; ++i) *(u32x4*)(smem + s * STAGE_B + ((ra + 16 * i) * 8 + pc) * 16) = ln8(av[i], mm[i], rr[i]);
        }
#pragma unroll
        for (int i = 0; i < 4; ++i) av[i] = *(const u32x4*)(ap[i] + (NST - 1) * BK);
        int buf = NST - 1;
        for (int kt = 0; kt < KSTEPS; ++kt) {
          RAW_BARRIER();
          const int n = kt + NST - 1;
          if (n < KSTEPS) {
#pragma unroll
            for (int i = 0; i < 4; ++i) *(u32x4*)(smem + buf * STAGE_B + ((ra + 16 * i) * 8 + pc) * 16) = ln8(av[i], mm[i], rr[i]);
            if (n + 1 < KSTEPS) {
#pragma unroll
              for (int i = 0; i < 4; ++i) av[i] = *(const u32x4*)(ap[i] + (n + 1) * BK);
            }
          }
          if (++buf == NST) buf = 0;
        }
      } else {
        int buf = NST - 1;
        for (int kt = 0; kt < KSTEPS; ++kt) {
          if (kt >= NST - 1) { if (kt <= KSTEPS - NST + 1) WAIT_VM((NST - 2) * NVM); else WAIT_VM(0); }
          RAW_BARRIER();
          const int n = kt + NST - 1;
          if (n < KSTEPS) issueB(n, buf);
          if (++buf == NST) buf = 0;
        }
      }
    } else {
#pragma unroll
      for (int s = 0; s < NST - 1; ++s) { issueA(s, s); issueB(s, s); }
      int buf = NST - 1;
      for (int kt = 0; kt < KSTEPS; ++kt) {
        if (kt <= KSTEPS - NST + 1) WAIT_VM((NST - 2) * NVM); else WAIT_VM(0);
        RAW_BARRIER();
        if (kt + NST - 1 < KSTEPS) { issueA(kt + NST - 1, buf); issueB(kt + NST - 1, buf); }
        if (++buf == NST) buf = 0;
      }
    }
  } else {
    const int nt0 = wn * NTW;
    const int ntw = (NT - nt0) < NTW ? (NT - nt0) : NTW;
    const int bt0 = (8 * g + 4 * (g & 1) + lq) * (SW * 2) + 8 * lp;
    const int bt1 = (8 * g + 4 * (1 ^ (g & 1)) + lq) * (SW * 2) + 8 * lp;
    int buf = 0;
    for (int kt = 0; kt < KSTEPS; ++kt) {
      RAW_BARRIER();
      const char* sb = smem + buf * STAGE_B;
#pragma unroll
      for (int ks = 0; ks < 2; ++ks) {
        bf16x8 af[2];
#pragma unroll
        for (int mt = 0; mt < 2; ++mt) {
          int r = wm * 32 + mt * 16 + li;
          int pc = (ks * 4 + g) ^ (r & 7);
          af[mt] = *(const bf16x8*)(sb + r * 128 + pc * 16);
        }
        const char* bb = sb + ACH * 16 + ks * 32 * (SW * 2);
#pragma unroll
        for (int nt = 0; nt < NTW; ++nt) {
          if (nt < ntw) {
            int ntg = nt0 + nt;
            int sub = ntg / NTS, n0 = (ntg % NTS) * 16;
            const char* bp = bb + sub * (BK * SW * 2) + n0 * 2;
            s16x4 b0 = trread((const u16*)(bp + bt0));
            s16x4 b1 = trread((const u16*)(bp + bt1));
            bf16x8 bf = cat4(b0, b1);
            acc[0][nt] = mfma16(af[0], bf, acc[0][nt]);
            acc[1][nt] = mfma16(af[1], bf, acc[1][nt]);
          }
        }
      }
      if (++buf == NST) buf = 0;
    }
  }
  __syncthreads();

  u16* Cs = (u16*)smem;
  if (!producer) {
    const int nt0 = wn * NTW;
    const int ntw = (NT - nt0) < NTW ? (NT - nt0) : NTW;
#pragma unroll
    for (int mt = 0; mt < 2; ++mt)
#pragma unroll
      for (int nt = 0; nt < NTW; ++nt)
        if (nt < ntw) {
#pragma unroll
          for (int i = 0; i < 4; ++i) {
            int r = wm * 32 + mt * 16 + 4 * g + i;
            int c = (nt0 + nt) * 16 + li;
            Cs[r * CSTR + c] = f2bf(acc[mt][nt][i]);
          }
        }
  }
  __syncthreads();
  if constexpr (EPI == 1) {
    if (!producer) {
#pragma unroll
      for (int i = 0; i < 3; ++i) {
        int c = i * 256 + tid;
        if (c < 640) {
          int r = c / 10, cc = c % 10;
          u32x4 v = *(const u32x4*)(Cs + r * CSTR + cc * 8), o;
#pragma unroll
          for (int j = 0; j < 4; ++j) {
            u16 lo = f2bf(bf2f(rres[i][j] & 0xffff) + bf2f(v[j] & 0xffff));
            u16 hi = f2bf(bf2f(rres[i][j] >> 16) + bf2f(v[j] >> 16));
            o[j] = (unsigned)lo | ((unsigned)hi << 16);
          }
          *(u32x4*)(out0 + (size_t)(row0 + r) * D_ + ct * BN + cc * 8) = o;
        }
      }
    }
  } else if constexpr (EPI == 2) {
#pragma unroll
    for (int i = 0; i < 3; ++i) {
      int c = i * 512 + tid;
      int r = c / 24, cc = c % 24;
      u32x4 v = *(const u32x4*)(Cs + r * CSTR + cc * 8), o;
#pragma unroll
      for (int j = 0; j < 4; ++j) {
        u16 lo = f2bf(gelu_f(bf2f(v[j] & 0xffff)));
        u16 hi = f2bf(gelu_f(bf2f(v[j] >> 16)));
        o[j] = (unsigned)lo | ((unsigned)hi << 16);
      }
      int col = ct * BN + cc * 8;
      if (col < FH_) *(u32x4*)(out0 + (size_t)(row0 + r) * FH_ + col) = o;
      else *(u32x4*)(out1 + (size_t)(row0 + r) * FH_ + (col - FH_)) = o;
    }
  } else {
    u16* PK = Cs + BM * CSTR;
    u16* PHI = PK + BM * 4 * 24;
    constexpr int PHS = 168;
    for (int i = tid; i < BM * 10; i += 512) {
      int r = i / 10, c = (i % 10) * 8;
      u32x4 v = *(const u32x4*)(Cs + r * CSTR + c);
      *(u32x4*)(out0 + (size_t)(row0 + r) * D_ + ct * 80 + c) = v;
    }
    {
      const int hw = w & 3, mh = w >> 2;
      const int h = ct * 4 + hw;
      bf16x8 wb[2];
#pragma unroll
      for (int n2 = 0; n2 < 2; ++n2) {
        int n = n2 * 16 + li;
#pragma unroll
        for (int j = 0; j < 8; ++j) {
          int k = 8 * g + j;
          u16 v = 0;
          if (k < DH_ && n < DH_) v = wphi[(size_t)h * DH_ * DH_ + k * DH_ + n];
          wb[n2][j] = (short)v;
        }
      }
#pragma unroll
      for (int mt2 = 0; mt2 < 2; ++mt2) {
        int mt = mh * 2 + mt2;
        const u16* ap = Cs + (mt * 16 + li) * CSTR + 80 + 20 * hw + 8 * g;
        u32x2 a0 = *(const u32x2*)ap, a1 = *(const u32x2*)(ap + 4);
        u32x4 av = {a0[0], a0[1], a1[0], a1[1]};
        bf16x8 af = __builtin_bit_cast(bf16x8, av);
#pragma unroll
        for (int n2 = 0; n2 < 2; ++n2) {
          f32x4 p = mfma16(af, wb[n2], f32x4{0.f, 0.f, 0.f, 0.f});
          int n = n2 * 16 + li;
          if (n < DH_) {
#pragma unroll
            for (int i = 0; i < 4; ++i) PK[((mt * 16 + 4 * g + i) * 4 + hw) * 24 + n] = f2bf(p[i]);
          }
        }
      }
    }
    __syncthreads();
    if (tid < 256) {
      const int r = tid >> 2, hh = tid & 3;
      const u16* pp = PK + (r * 4 + hh) * 24;
      float p[20];
#pragma unroll
      for (int j = 0; j < 20; ++j) p[j] = bf2f(pp[j]);
      float mx = p[0], mn = p[0];
#pragma unroll
      for (int j = 1; j < 20; ++j) { mx = fmaxf(mx, p[j]); mn = fminf(mn, p[j]); }
      float e1[20], e2[20], s1 = 0.f, s2 = 0.f;
#pragma unroll
      for (int j = 0; j < 20; ++j) {
        e1[j] = __expf(p[j] - mx); s1 += e1[j];
        e2[j] = __expf(mn - p[j]); s2 += e2[j];
      }
      float r1 = 1.0f / s1, r2 = 1.0f / s2;
      u16* dst = PHI + r * PHS + hh * 40;
#pragma unroll
      for (int j = 0; j < 20; ++j) { dst[j] = f2bf(e1[j] * r1); dst[20 + j] = f2bf(e2[j] * r2); }
    }
    __syncthreads();
    if (w < 4) {
      const int h = ct * 4 + w;
      f32x4 kv[3][2];
#pragma unroll
      for (int a = 0; a < 3; ++a)
#pragma unroll
        for (int b = 0; b < 2; ++b) kv[a][b] = f32x4{0.f, 0.f, 0.f, 0.f};
#pragma unroll
      for (int ks = 0; ks < 2; ++ks) {
        bf16x8 vb[2];
#pragma unroll
        for (int n2 = 0; n2 < 2; ++n2) {
          const u16* bp = Cs + (ks * 32 + 8 * g + lq) * CSTR + 160 + 20 * w + n2 * 16 + 4 * lp;
          s16x4 b0 = trread(bp), b1 = trread(bp + 4 * CSTR);
          bf16x8 bf = cat4(b0, b1);
          if (n2 == 1) {
            short fill = (li == 4) ? (short)0x3F80 : (short)0;
            if (li >= 4) {
#pragma unroll
              for (int j = 0; j < 8; ++j) bf[j] = fill;
            }
          }
          vb[n2] = bf;
        }
#pragma unroll
        for (int mt = 0; mt < 3; ++mt) {
          const u16* ap = PHI + (ks * 32 + 8 * g + lq) * PHS + 40 * w + mt * 16 + 4 * lp;
          s16x4 a0 = trread(ap), a1 = trread(ap + 4 * PHS);
          bf16x8 af = cat4(a0, a1);
#pragma unroll
          for (int n2 = 0; n2 < 2; ++n2) kv[mt][n2] = mfma16(af, vb[n2], kv[mt][n2]);
        }
      }
      float* dst = kvpart + ((size_t)mt_blk * H_ + h) * 960;
#pragma unroll
      for (int mt = 0; mt < 3; ++mt)
#pragma unroll
        for (int n2 = 0; n2 < 2; ++n2) {
          int d = n2 * 16 + li;
          if (d < 21) {
#pragma unroll
            for (int i = 0; i < 4; ++i) {
              int f = mt * 16 + 4 * g + i;
              if (f < 40) dst[f * 24 + d] = kv[mt][n2][i];
            }
          }
        }
    }
  }
}

__global__ void __launch_bounds__(256)
ln_x(const u16* __restrict__ x, u16* __restrict__ xn) {
  const int tid = threadIdx.x, r = blockIdx.x * 8 + (tid >> 5), l = tid & 31;
  const u16* xr = x + (size_t)r * D_;
  u32x4 xv[3];
#pragma unroll
  for (int i = 0; i < 3; ++i) { int c = l + 32 * i; xv[i] = (c < 80) ? *(const u32x4*)(xr + c * 8) : u32x4{0u, 0u, 0u, 0u}; }
  float s = 0.f;
#pragma unroll
  for (int i = 0; i < 3; ++i)
#pragma unroll
    for (int j = 0; j < 4; ++j) { s += bf2f(xv[i][j] & 0xffff); s += bf2f(xv[i][j] >> 16); }
#pragma unroll
  for (int o = 1; o < 32; o <<= 1) s += __shfl_xor(s, o);
  const float mean = s * (1.0f / D_);
  float v = 0.f;
#pragma unroll
  for (int i = 0; i < 3; ++i) {
    if (l + 32 * i < 80) {
#pragma unroll
      for (int j = 0; j < 4; ++j) {
        float a = bf2f(xv[i][j] & 0xffff) - mean, b = bf2f(xv[i][j] >> 16) - mean;
        v += a * a; v += b * b;
      }
    }
  }
#pragma unroll
  for (int o = 1; o < 32; o <<= 1) v += __shfl_xor(v, o);
  const float m_bf = rbf(mean);
  const float rs = 1.0f / rbf(sqrtf(rbf(rbf(v * (1.0f / D_)) + 1e-5f)));
#pragma unroll
  for (int i = 0; i < 3; ++i) { int c = l + 32 * i; if (c < 80) *(u32x4*)(xn + (size_t)r * D_ + c * 8) = ln8(xv[i], m_bf, rs); }
}

__global__ void __launch_bounds__(256)
attn_apply(const u16* __restrict__ q, const u16* __restrict__ wphi, const float* __restrict__ kvpart,
           u16* __restrict__ o) {
  __shared__ __attribute__((aligned(16))) u16 KVZ[32 * 72];
  __shared__ __attribute__((aligned(16))) u16 PQ[256 * 24];
  __shared__ __attribute__((aligned(16))) u16 PHI[256 * 72];
  const int tid = threadIdx.x, lane = tid & 63, w = tid >> 6;
  const int h = blockIdx.x & 31, rb = blockIdx.x >> 5;
  const int g = lane >> 4, li = lane & 15;
  const int row0 = rb * 256;
  for (int i = tid; i < 32 * 72 / 2; i += 256) ((unsigned*)KVZ)[i] = 0;
  f32x4 kvv[32];
  if (tid < 240) {
    const f32x4* src = (const f32x4*)(kvpart + (size_t)h * 960) + tid;
#pragma unroll
    for (int r = 0; r < 32; ++r) kvv[r] = src[(size_t)r * (H_ * 960 / 4)];
  }
  u32x4 qa[4];
#pragma unroll
  for (int mt = 0; mt < 4; ++mt) {
    int r = w * 64 + mt * 16 + li;
    const u16* ap = q + (size_t)(row0 + r) * D_ + h * DH_ + 8 * g;
    u32x4 av = {0u, 0u, 0u, 0u};
    if (g < 2) { u32x2 a0 = *(const u32x2*)ap, a1 = *(const u32x2*)(ap + 4); av = u32x4{a0[0], a0[1], a1[0], a1[1]}; }
    else if (g == 2) { u32x2 a0 = *(const u32x2*)ap; av[0] = a0[0]; av[1] = a0[1]; }
    qa[mt] = av;
  }
  bf16x8 wb[2];
#pragma unroll
  for (int n2 = 0; n2 < 2; ++n2) {
    int n = n2 * 16 + li;
#pragma unroll
    for (int j = 0; j < 8; ++j) {
      int k = 8 * g + j;
      u16 v = 0;
      if (k < DH_ && n < DH_) v = wphi[(size_t)h * DH_ * DH_ + k * DH_ + n];
      wb[n2][j] = (short)v;
    }
  }
  __syncthreads();
  if (tid < 240) {
    f32x4 s = {0.f, 0.f, 0.f, 0.f};
#pragma unroll
    for (int r = 0; r < 32; ++r) s += kvv[r];
    int f = tid / 6, d0 = (tid % 6) * 4;
#pragma unroll
    for (int j = 0; j < 4; ++j) if (d0 + j < 21) KVZ[(d0 + j) * 72 + f] = f2bf(s[j]);
  }
#pragma unroll
  for (int mt = 0; mt < 4; ++mt) {
    bf16x8 af = __builtin_bit_cast(bf16x8, qa[mt]);
#pragma unroll
    for (int n2 = 0; n2 < 2; ++n2) {
      f32x4 p = mfma16(af, wb[n2], f32x4{0.f, 0.f, 0.f, 0.f});
      int n = n2 * 16 + li;
      if (n < DH_) {
#pragma unroll
        for (int i = 0; i < 4; ++i) PQ[(w * 64 + mt * 16 + 4 * g + i) * 24 + n] = f2bf(p[i]);
      }
    }
  }
  __syncthreads();
  {
    const int r = tid;
    const u16* pp = PQ + r * 24;
    float p[20];
#pragma unroll
    for (int j = 0; j < 20; ++j) p[j] = bf2f(pp[j]);
    float mx = p[0], mn = p[0];
#pragma unroll
    for (int j = 1; j < 20; ++j) { mx = fmaxf(mx, p[j]); mn = fminf(mn, p[j]); }
    float e1[20], e2[20], s1 = 0.f, s2 = 0.f;
#pragma unroll
    for (int j = 0; j < 20; ++j) {
      e1[j] = __expf(p[j] - mx); s1 += e1[j];
      e2[j] = __expf(mn - p[j]); s2 += e2[j];
    }
    float r1 = 1.0f / s1, r2 = 1.0f / s2;
    u16* dst = PHI + r * 72;
#pragma unroll
    for (int j = 0; j < 20; ++j) { dst[j] = f2bf(e1[j] * r1); dst[20 + j] = f2bf(e2[j] * r2); }
#pragma unroll
    for (int j = 40; j < 64; j += 2) *(unsigned*)(dst + j) = 0u;
  }
  __syncthreads();
  {
    bf16x8 kb[2][2];
#pragma unroll
    for (int ks = 0; ks < 2; ++ks)
#pragma unroll
      for (int n2 = 0; n2 < 2; ++n2)
        kb[ks][n2] = *(const bf16x8*)(KVZ + (n2 * 16 + li) * 72 + ks * 32 + 8 * g);
#pragma unroll
    for (int mt = 0; mt < 4; ++mt) {
      int r = w * 64 + mt * 16 + li;
      f32x4 num0 = {0.f, 0.f, 0.f, 0.f}, num1 = {0.f, 0.f, 0.f, 0.f};
#pragma unroll
      for (int ks = 0; ks < 2; ++ks) {
        bf16x8 af = *(const bf16x8*)(PHI + r * 72 + ks * 32 + 8 * g);
        num0 = mfma16(af, kb[ks][0], num0);
        num1 = mfma16(af, kb[ks][1], num1);
      }
#pragma unroll
      for (int i = 0; i < 4; ++i) {
        float den = __shfl(num1[i], g * 16 + 4);
        den = __builtin_amdgcn_rcpf(rbf(rbf(den) + 1e-6f));
        int rr = w * 64 + mt * 16 + 4 * g + i;
        PQ[rr * 24 + li] = f2bf(rbf(num0[i]) * den);
        if (li < 4) PQ[rr * 24 + 16 + li] = f2bf(rbf(num1[i]) * den);
      }
    }
  }
  __syncthreads();
  for (int i = tid; i < 256 * 5; i += 256) {
    int r = i / 5, p = i % 5;
    u32x2 v = *(const u32x2*)(PQ + r * 24 + p * 4);
    *(u32x2*)(o + (size_t)(row0 + r) * D_ + h * DH_ + p * 4) = v;
  }
}

__global__ void __launch_bounds__(256)
ln_z2_t(const u16* __restrict__ z2raw, u16* __restrict__ z2nT) {
  __shared__ float stat[64][2];
  __shared__ __attribute__((aligned(16))) u16 T[96 * 72];
  const int tid = threadIdx.x;
  const int rb = blockIdx.x >> 3, cs = blockIdx.x & 7;
  const int row0 = rb * 64;
  {
    const int r = tid >> 2, part = tid & 3;
    const u16* zr = z2raw + (size_t)(row0 + r) * FH_;
    u32x4 xv[24];
#pragma unroll
    for (int i = 0; i < 24; ++i) xv[i] = *(const u32x4*)(zr + (part + 4 * i) * 8);
    float s = 0.f;
#pragma unroll
    for (int i = 0; i < 24; ++i)
#pragma unroll
      for (int j = 0; j < 4; ++j) { s += bf2f(xv[i][j] & 0xffff); s += bf2f(xv[i][j] >> 16); }
    s += __shfl_xor(s, 1); s += __shfl_xor(s, 2);
    const float mean = s * (1.0f / FH_);
    float v = 0.f;
#pragma unroll
    for (int i = 0; i < 24; ++i)
#pragma unroll
      for (int j = 0; j < 4; ++j) {
        float a = bf2f(xv[i][j] & 0xffff) - mean, b = bf2f(xv[i][j] >> 16) - mean;
        v += a * a; v += b * b;
      }
    v += __shfl_xor(v, 1); v += __shfl_xor(v, 2);
    if (part == 0) {
      stat[r][0] = rbf(mean);
      stat[r][1] = 1.0f / rbf(sqrtf(rbf(rbf(v * (1.0f / FH_)) + 1e-5f)));
    }
  }
  __syncthreads();
  for (int i = tid; i < 64 * 12; i += 256) {
    int r = i / 12, c = (i % 12) * 8;
    u32x4 xv = *(const u32x4*)(z2raw + (size_t)(row0 + r) * FH_ + cs * 96 + c);
    float m = stat[r][0], rs = stat[r][1];
#pragma unroll
    for (int j = 0; j < 4; ++j) {
      T[(c + 2 * j) * 72 + r] = f2bf(ln_apply(bf2f(xv[j] & 0xffff), m, rs));
      T[(c + 2 * j + 1) * 72 + r] = f2bf(ln_apply(bf2f(xv[j] >> 16), m, rs));
    }
  }
  __syncthreads();
  for (int i = tid; i < 96 * 8; i += 256) {
    int f = i >> 3, c = (i & 7) * 8;
    u32x4 v = *(const u32x4*)(T + f * 72 + c);
    *(u32x4*)(z2nT + (size_t)(cs * 96 + f) * S_ + row0 + c) = v;
  }
}

template <int BK, int NST>
__global__ void __launch_bounds__(512)
gemm_sp(const u16* __restrict__ wsp, const u16* __restrict__ z2nT, const u16* __restrict__ bsp,
        const u16* __restrict__ z1, u16* __restrict__ hout) {
  constexpr int BN = 96, CPR = BK / 8;
  constexpr int ACH = BM * CPR, BCH = BN * CPR, NI = (ACH + BCH) / 256;
  static_assert((ACH + BCH) % 256 == 0 && ACH % 256 == 0, "");
  constexpr int STAGE_B = NI * 256 * 16;
  constexpr int CSTR = BN + 8;
  __shared__ __attribute__((aligned(16))) char smem[NST * STAGE_B];
  const int tid = threadIdx.x, lane = tid & 63, w = tid >> 6;
  const bool producer = (w >= 4);
  const int wm = w & 1, wn = (w >> 1) & 1;
  const int mb = (blockIdx.x & 7) * 4 + ((blockIdx.x >> 3) & 3), nb = blockIdx.x >> 5;
  const int row0 = mb * BM, col0 = nb * BN;
  const int g = lane >> 4, li = lane & 15;
  const int pt = tid - 256;
  constexpr int KSTEPS = S_ / BK;
  const int kshift = (mb * 5 + nb * 3) & (KSTEPS - 1);
  f32x4 acc[2][3];
#pragma unroll
  for (int a = 0; a < 2; ++a)
#pragma unroll
    for (int b = 0; b < 3; ++b) acc[a][b] = f32x4{0.f, 0.f, 0.f, 0.f};
  u32x4 zr[3]; float bb[3];
  if (!producer) {
#pragma unroll
    for (int i = 0; i < 3; ++i) { int c = i * 256 + tid; int r = c / 12, cc = c % 12; zr[i] = *(const u32x4*)(z1 + (size_t)(row0 + r) * FH_ + col0 + cc * 8); bb[i] = bf2f(bsp[row0 + r]); }
  }
  if (producer) {
    const u16* src[NI];
#pragma unroll
    for (int i = 0; i < NI; ++i) {
      int c = i * 256 + pt;
      if (i < ACH / 256) { int r = c / CPR, pc = c % CPR; src[i] = wsp + (size_t)(row0 + r) * S_ + (((pc & ~7) | ((pc ^ r) & 7)) * 8); }
      else { int b = c - ACH; int r = b / CPR, pc = b % CPR; src[i] = z2nT + (size_t)(col0 + r) * S_ + (((pc & ~7) | ((pc ^ r) & 7)) * 8); }
    }
    auto issue = [&](int kt, int buf) {
#pragma unroll
      for (int i = 0; i < NI; ++i) dma16(src[i] + (((kt + kshift) & (KSTEPS - 1)) * BK), smem + buf * STAGE_B + (i * 256 + pt) * 16);
    };
#pragma unroll
    for (int s = 0; s < NST - 1; ++s) issue(s, s);
    int buf = NST - 1;
    for (int kt = 0; kt < KSTEPS; ++kt) {
      if (kt <= KSTEPS - NST + 1) WAIT_VM((NST - 2) * NI); else WAIT_VM(0);
      RAW_BARRIER();
      if (kt + NST - 1 < KSTEPS) issue(kt + NST - 1, buf);
      if (++buf == NST) buf = 0;
    }
  } else {
    int buf = 0;
    for (int kt = 0; kt < KSTEPS; ++kt) {
      RAW_BARRIER();
      const char* sa = smem + buf * STAGE_B;
      const char* sbp = sa + ACH * 16;
#pragma unroll
      for (int ks = 0; ks < BK / 32; ++ks) {
        bf16x8 af[2], bf[3];
        int c = ks * 4 + g;
#pragma unroll
        for (int mt = 0; mt < 2; ++mt) { int r = wm * 32 + mt * 16 + li; af[mt] = *(const bf16x8*)(sa + r * (BK * 2) + (((c & ~7) | ((c ^ r) & 7)) * 16)); }
#pragma unroll
        for (int nt = 0; nt < 3; ++nt) { int r = wn * 48 + nt * 16 + li; bf[nt] = *(const bf16x8*)(sbp + r * (BK * 2) + (((c & ~7) | ((c ^ r) & 7)) * 16)); }
#pragma unroll
        for (int mt = 0; mt < 2; ++mt)
#pragma unroll
          for (int nt = 0; nt < 3; ++nt) acc[mt][nt] = mfma16(af[mt], bf[nt], acc[mt][nt]);
      }
      if (++buf == NST) buf = 0;
    }
  }
  __syncthreads();
  u16* Cs = (u16*)smem;
  if (!producer) {
#pragma unroll
    for (int mt = 0; mt < 2; ++mt)
#pragma unroll
      for (int nt = 0; nt < 3; ++nt)
#pragma unroll
        for (int i = 0; i < 4; ++i) Cs[(wm * 32 + mt * 16 + 4 * g + i) * CSTR + wn * 48 + nt * 16 + li] = f2bf(acc[mt][nt][i]);
  }
  __syncthreads();
  if (!producer) {
#pragma unroll
    for (int i = 0; i < 3; ++i) {
      int c = i * 256 + tid; int r = c / 12, cc = c % 12;
      u32x4 v = *(const u32x4*)(Cs + r * CSTR + cc * 8), o;
#pragma unroll
      for (int j = 0; j < 4; ++j) {
        u16 lo = f2bf(bf2f(zr[i][j] & 0xffff) * rbf(bf2f(v[j] & 0xffff) + bb[i]));
        u16 hi = f2bf(bf2f(zr[i][j] >> 16) * rbf(bf2f(v[j] >> 16) + bb[i]));
        o[j] = (unsigned)lo | ((unsigned)hi << 16);
      }
      *(u32x4*)(hout + (size_t)(row0 + r) * FH_ + col0 + cc * 8) = o;
    }
  }
}

extern "C" void kernel_launch(void* const* d_in, const int* in_sizes, int n_in,
                              void* d_out, int out_size, void* d_ws, size_t ws_size,
                              hipStream_t stream) {
  const u16* A_x = (const u16*)d_in[0];
  const u16* wqkv = (const u16*)d_in[1];
  const u16* wphi = (const u16*)d_in[2];
  const u16* wo = (const u16*)d_in[3];
  const u16* w_in = (const u16*)d_in[5];
  const u16* w_sp = (const u16*)d_in[6];
  const u16* b_sp = (const u16*)d_in[7];
  const u16* w_out = (const u16*)d_in[8];
  u16* out = (u16*)d_out;

  char* ws = (char*)d_ws;
  size_t off = 0;
  auto alloc = [&](size_t bytes) { char* p = ws + off; off += (bytes + 255) & ~(size_t)255; return p; };
  u16* xa = (u16*)alloc((size_t)S_ * D_ * 2);
  u16* xb = (u16*)alloc((size_t)S_ * D_ * 2);
  u16* xn = (u16*)alloc((size_t)S_ * D_ * 2);
  u16* qb = (u16*)alloc((size_t)S_ * D_ * 2);
  u16* ob = (u16*)alloc((size_t)S_ * D_ * 2);
  float* kvp = (float*)alloc((size_t)32 * H_ * 960 * 4);
  u16* z1 = (u16*)alloc((size_t)S_ * FH_ * 2);
  u16* z2r = (u16*)alloc((size_t)S_ * FH_ * 2);
  u16* z2t = (u16*)alloc((size_t)S_ * FH_ * 2);
  u16* hb = (u16*)alloc((size_t)S_ * FH_ * 2);

  const u16* xin = A_x;
  for (int rep = 0; rep < 2; ++rep) {
    u16* x1 = xa;
    u16* x2 = (rep == 0) ? xb : out;
    ln_x<<<256, 256, 0, stream>>>(xin, xn);
    gemm_nn<0, false, 240, 80, 640, 3><<<256, 512, 0, stream>>>(xn, wqkv, 3 * D_, nullptr, qb, nullptr, wphi, kvp);
    attn_apply<<<256, 256, 0, stream>>>(qb, wphi, kvp, ob);
    gemm_nn<1, false, 80, 80, 640, 4><<<256, 512, 0, stream>>>(ob, wo, D_, xin, x1, nullptr, nullptr, nullptr);
    ln_x<<<256, 256, 0, stream>>>(x1, xn);
    gemm_nn<2, false, 192, 48, 640, 4><<<256, 512, 0, stream>>>(xn, w_in, 2 * FH_, nullptr, z1, z2r, nullptr, nullptr);
    ln_z2_t<<<256, 256, 0, stream>>>(z2r, z2t);
    gemm_sp<64, 4><<<256, 512, 0, stream>>>(w_sp, z2t, b_sp, z1, hb);
    gemm_nn<1, false, 80, 80, 768, 4><<<256, 512, 0, stream>>>(hb, w_out, D_, x1, x2, nullptr, nullptr, nullptr);
    xin = x2;
  }
}
```
